```python
import torch

def _softmax(x, axis=-1):
    return torch.softmax(x, dim=axis)
def _gelu(x):
    return torch.nn.functional.gelu(x, approximate="tanh")
def _silu(x):
    return torch.nn.functional.silu(x)
def _sigmoid(x):
    return torch.sigmoid(x)
def _layernorm(x, axis=-1, eps=1e-5):
    m = x.mean(dim=axis, keepdim=True)
    v = x.var(dim=axis, keepdim=True, unbiased=False)
    return (x - m) / torch.sqrt(v + eps)
def _scatter_add(tgt, idx, src, axis=0):
    return tgt.clone().index_add_(axis, idx, src)
def _segment_sum(src, seg, n):
    out = torch.zeros((n,) + src.shape[1:], dtype=src.dtype)
    return out.index_add_(0, seg, src)
def _pad(x, pw):
    flat = tuple(v for pair in reversed(pw) for v in pair)
    return torch.nn.functional.pad(x, flat)

kernel_name = 'gated_mix__ssm_conv_s4__linear_attn_block__prompt'

def setup_inputs():

    _w = (lambda a: (lambda t: t.to(torch.bfloat16) if t.is_floating_point() else t)(torch.as_tensor(a)))
    def _A_setup():
        B, L, D, N = 1, 2048, 640, 192
        A_re = -(lambda _x: torch.exp(torch.as_tensor(_x)))((lambda *s: torch.randn(*s, dtype=torch.bfloat16) * 6.4)(D, N))
        A_im = torch.pi * (lambda *s: torch.randn(*s, dtype=torch.bfloat16) * 0.05)(D, N)
        B_re = (lambda *s: torch.randn(*s, dtype=torch.bfloat16) * 0.1)(D, N)
        B_im = (lambda *s: torch.randn(*s, dtype=torch.bfloat16) * 0.1)(D, N)
        C_re = (lambda *s: torch.randn(*s, dtype=torch.bfloat16) * 0.1)(D, N)
        C_im = (lambda *s: torch.randn(*s, dtype=torch.bfloat16) * 0.1)(D, N)
        log_dt = (lambda *s: torch.randn(*s, dtype=torch.bfloat16) * 0.05)(D) * 0.5 - 4.0
        Dskip = (lambda *s: torch.randn(*s, dtype=torch.bfloat16) * 0.05)(D)
        return {
            "u": _w((lambda *s: torch.randn(*s, dtype=torch.bfloat16) * 0.05)(B, L, D)),
            "A_re": _w(A_re),
            "A_im": _w(A_im),
            "B_re": _w(B_re),
            "B_im": _w(B_im),
            "C_re": _w(C_re),
            "C_im": _w(C_im),
            "log_dt": _w(log_dt),
            "Dskip": _w(Dskip),
        }
    _A_in = _A_setup()

    def _B_setup():
        B, S, D = 1, 2048, 640
        return {
            "x": _w((lambda *s: torch.randn(*s, dtype=torch.bfloat16) * 0.05)(B, S, D)),
            "wqkv": _w((lambda *s: torch.randn(*s, dtype=torch.bfloat16) * 0.4)(D, 3 * D)),
            "wo": _w((lambda *s: torch.randn(*s, dtype=torch.bfloat16) * 0.05)(D, D)),
        }
    _B_in = _B_setup()

    return {
        "A_u": _A_in["u"],
        "A_A_re": _A_in["A_re"],
        "A_A_im": _A_in["A_im"],
        "A_B_re": _A_in["B_re"],
        "A_B_im": _A_in["B_im"],
        "A_C_re": _A_in["C_re"],
        "A_C_im": _A_in["C_im"],
        "A_log_dt": _A_in["log_dt"],
        "A_Dskip": _A_in["Dskip"],
        "B_x": _B_in["x"],
        "B_wqkv": _B_in["wqkv"],
        "B_wo": _B_in["wo"],
        "gate_w": _w((lambda *s: torch.randn(*s, dtype=torch.bfloat16) * 0.05)(640) * 16.0),
    }

def reference(A_u, A_A_re, A_A_im, A_B_re, A_B_im, A_C_re, A_C_im, A_log_dt, A_Dskip, B_x, B_wqkv, B_wo, gate_w):
    def _A_ref(u, A_re, A_im, B_re, B_im, C_re, C_im, log_dt, Dskip):
        Bsz, L, D = u.shape
        dt = torch.exp(log_dt)[:, None]
        A = A_re + 1j * A_im
        Bc = B_re + 1j * B_im
        Cc = C_re + 1j * C_im
        logdA = dt * A
        dB = dt * Bc
        l = (lambda x, dt: x.to(dt))(torch.arange(L), A_re.dtype)
        P = torch.exp(logdA[:, :, None] * l[None, None, :])
        K = torch.einsum("dn,dnl,dn->dl", Cc, P, dB).real
        L2 = 2 * L
        Kp = _pad(K, ((0, 0), (0, L)))
        up = (lambda x, *d: x.permute(*d))(u, 0, 2, 1)
        up = _pad(up, ((0, 0), (0, 0), (0, L)))
        Kf = (lambda x, axes=(-2,-1): torch.fft.rfft2(x.float(), dim=axes))(Kp[:, None, :], axes=(-2, -1))
        Uf = (lambda x, axes=(-2,-1): torch.fft.rfft2(x.float(), dim=axes))(up[:, :, None, :], axes=(-2, -1))
        Yf = Uf * Kf[None, :, :, :]
        y = (lambda x, s, axes=(-2,-1): torch.fft.irfft2(x, s=s, dim=axes).to(torch.bfloat16))(Yf, (1, L2), axes=(-2, -1))[:, :, 0, :L]
        y = (lambda x, *d: x.permute(*d))(y, 0, 2, 1) + u * Dskip[None, None, :]
        return y

    def _B_ref(x, wqkv, wo):
        B, S, D = x.shape
        H = 32; Dh = D // H
        qkv = (lambda x, *d: x.permute(*d))((x @ wqkv).reshape(B, S, 3, H, Dh), 2, 0, 3, 1, 4)
        q, k, v = qkv[0], qkv[1], qkv[2]
        phi = lambda t: torch.where(t > 0, t + 1, torch.exp(t)) + 1e-6
        pq, pk = phi(q), phi(k)
        kv = torch.einsum("bhsd,bhse->bhde", pk, v)
        z = torch.einsum("bhsd->bhd", pk)
        num = torch.einsum("bhqd,bhde->bhqe", pq, kv)
        den = torch.einsum("bhqd,bhd->bhq", pq, z)[..., None] + 1e-6
        o = num / den
        return (lambda x, *d: x.permute(*d))(o, 0, 2, 1, 3).reshape(B, S, D) @ wo

    x = A_u
    g = _sigmoid((x * gate_w).sum(-1, keepdims=True))
    a = _A_ref(x, A_A_re, A_A_im, A_B_re, A_B_im, A_C_re, A_C_im, A_log_dt, A_Dskip)
    b = _B_ref(x, B_wqkv, B_wo)
    return g * a + (1 - g) * b
```

```cpp
#include <hip/hip_runtime.h>
#include <hip/hip_bf16.h>
#include <stdint.h>

typedef unsigned short u16;
typedef __attribute__((ext_vector_type(8))) __bf16 bf16x8;
typedef __attribute__((ext_vector_type(4))) __bf16 bf16x4;
typedef __attribute__((ext_vector_type(2))) __bf16 bf16x2;
typedef __attribute__((ext_vector_type(2))) float f32x2;
typedef __attribute__((ext_vector_type(4))) float f32x4;
typedef __attribute__((ext_vector_type(16))) float f32x16;
typedef __attribute__((ext_vector_type(2))) unsigned u32x2;
typedef __attribute__((ext_vector_type(4))) unsigned u32x4;
#define LAS __attribute__((address_space(3)))
#define GAS __attribute__((address_space(1)))
#if defined(__HIP_DEVICE_COMPILE__)
#define NOPK __attribute__((target("no-packed-fp32-ops")))
#else
#define NOPK
#endif
#define DEV __device__ __forceinline__ NOPK
#define LAM __attribute__((always_inline)) NOPK

constexpr int SL = 2048;
constexpr int DM = 640;
constexpr int NM = 192;
constexpr int RSTR = 2080;

constexpr size_t WS_PART = 0;
constexpr size_t WS_PQ   = 1u << 20;
constexpr size_t WS_R    = WS_PQ + (size_t)SL * DM * 2;
constexpr size_t WS_UT   = WS_R + (size_t)DM * RSTR * 2;
constexpr size_t WS_AT   = WS_UT + (size_t)SL * DM * 2;
constexpr size_t WS_O    = WS_AT + (size_t)SL * DM * 2;
constexpr size_t WS_G    = WS_O + (size_t)SL * DM * 2;

DEV float bf2f(unsigned h) { return __builtin_bit_cast(float, h << 16); }
DEV float bflo(unsigned w) { return __builtin_bit_cast(float, w << 16); }
DEV float bfhi(unsigned w) { return __builtin_bit_cast(float, w & 0xffff0000u); }
DEV int tidx() { return __builtin_amdgcn_workitem_id_x(); }
DEV int bidx() { return __builtin_amdgcn_workgroup_id_x(); }
DEV float exp_acc(float x) {
  const float hi = x * 1.4426950408889634f;
  const float lo = __builtin_fmaf(x, 1.4426950408889634f, -hi) + x * 1.9259629911e-8f;
  const float e = __builtin_amdgcn_exp2f(hi);
  return __builtin_fmaf(e, lo * 0.6931471805599453f, e);
}
DEV float xor_sum(float s) {
#pragma unroll
  for (int m = 32; m >= 1; m >>= 1) {
    const int src = ((__builtin_amdgcn_mbcnt_hi(~0u, __builtin_amdgcn_mbcnt_lo(~0u, 0u)) ^ m) << 2);
    s += __builtin_bit_cast(float, __builtin_amdgcn_ds_bpermute(src, __builtin_bit_cast(int, s)));
  }
  return s;
}
DEV unsigned pk2(float a, float b) {
  f32x2 v = {a, b};
  return __builtin_bit_cast(unsigned, __builtin_convertvector(v, bf16x2));
}
DEV float rbf(float a) { return bflo(pk2(a, a)); }
DEV bf16x8 mk8(unsigned a, unsigned b, unsigned c, unsigned d) {
  u32x4 v = {a, b, c, d};
  return __builtin_bit_cast(bf16x8, v);
}
DEV f32x4 mfma16(bf16x8 a, bf16x8 b, f32x4 c) { return __builtin_amdgcn_mfma_f32_16x16x32_bf16(a, b, c, 0, 0, 0); }
DEV f32x16 mfma32(bf16x8 a, bf16x8 b, f32x16 c) { return __builtin_amdgcn_mfma_f32_32x32x16_bf16(a, b, c, 0, 0, 0); }
DEV bf16x8 lds_b128(const char* smem, unsigned off) { return *(const LAS bf16x8*)((const LAS char*)smem + off); }
DEV bf16x8 lds_tr2(const char* smem, unsigned off0, unsigned off1) {
  bf16x4 t0 = __builtin_amdgcn_ds_read_tr16_b64_v4bf16((LAS bf16x4*)((LAS char*)smem + off0));
  bf16x4 t1 = __builtin_amdgcn_ds_read_tr16_b64_v4bf16((LAS bf16x4*)((LAS char*)smem + off1));
  return __builtin_shufflevector(t0, t1, 0, 1, 2, 3, 4, 5, 6, 7);
}
DEV void glds16(const void* g, char* smem, unsigned off) {
  __builtin_amdgcn_global_load_lds((const GAS unsigned*)g, (LAS unsigned*)((LAS char*)smem + off), 16, 0, 0);
}
#define FENCE() asm volatile("" ::: "memory")
#define WAIT_VL(n) asm volatile("s_waitcnt vmcnt(" #n ") lgkmcnt(0)" ::: "memory")
DEV void bar() { FENCE(); __builtin_amdgcn_s_barrier(); FENCE(); }
template <int N> DEV void wait_vm() { asm volatile("s_waitcnt vmcnt(%0) lgkmcnt(0)" ::"i"(N) : "memory"); }

DEV float phi(float t) {
  float e = __builtin_amdgcn_exp2f(t * 1.4426950408889634f);
  e = __builtin_amdgcn_fmed3f(e, 0.f, 1.f);
  return e + (__builtin_fmaxf(t, 0.f) + 1e-6f);
}

template <int NBUF>
DEV void qkv_role(int b, const u16* __restrict__ x, const u16* __restrict__ w, float* __restrict__ part,
                  u16* __restrict__ pq, char* smem) {
  const int tid = tidx(), lane = tid & 63;
  const int wv = __builtin_amdgcn_readfirstlane(tid >> 6);
  const int xcd = b & 7, ii = b >> 3;
  const int rt = 4 * (xcd >> 1) + (ii >> 3), g = 8 * (xcd & 1) + (ii & 7);

  const u16* xsrc[4];
  const u16* wsrc[4];
#pragma unroll
  for (int q = 0; q < 4; ++q) {
    const int row = 32 * wv + 8 * q + (lane >> 3);
    const int ch = (lane & 7) ^ ((row >> 1) & 7);
    xsrc[q] = x + (size_t)(rt * 128 + row) * DM + ch * 8;
    const int kr = 16 * wv + 4 * q + (lane >> 4);
    int wc = (lane & 15) ^ (((kr & 3) << 2) | ((kr >> 2) & 3));
    wc = wc > 14 ? 14 : wc;
    const int seg = wc / 5;
    const int col = (seg == 0 ? DM : (seg == 1 ? 2 * DM : 0)) + 40 * g + (wc - seg * 5) * 8;
    wsrc[q] = w + (size_t)kr * (3 * DM) + col;
  }
  auto issue = [&](int kt) LAM {
    const unsigned base = (kt % NBUF) * 32768;
#pragma unroll
    for (int q = 0; q < 4; ++q) glds16(xsrc[q] + kt * 64, smem, base + wv * 4096 + q * 1024);
#pragma unroll
    for (int q = 0; q < 4; ++q) glds16(wsrc[q] + (size_t)kt * 64 * (3 * DM), smem, base + 16384 + (4 * wv + q) * 1024);
  };

  const int c16 = lane & 15, g4 = lane >> 4;
  unsigned aoff[2];
  {
    const int row = 32 * wv + c16;
#pragma unroll
    for (int ks = 0; ks < 2; ++ks) aoff[ks] = row * 128 + 16 * ((4 * ks + g4) ^ ((row >> 1) & 7));
  }
  unsigned boff[2];
  {
    const int q = c16 >> 2, p = lane & 3;
#pragma unroll
    for (int t = 0; t < 2; ++t) {
      const int kr = 8 * g4 + 4 * t + q;
      const int f = ((kr & 3) << 2) | ((kr >> 2) & 3);
      boff[t] = 16384 + kr * 256 + 16 * ((p >> 1) ^ f) + 8 * (p & 1);
    }
  }

  f32x4 acc[2][8];
#pragma unroll
  for (int m = 0; m < 2; ++m)
#pragma unroll
    for (int n = 0; n < 8; ++n) acc[m][n] = (f32x4){0.f, 0.f, 0.f, 0.f};

#pragma unroll
  for (int s = 0; s < NBUF - 1; ++s) issue(s);

#pragma unroll
  for (int kt = 0; kt < 10; ++kt) {
    constexpr int MAXF = NBUF - 2;
    const int later = (9 - kt) < MAXF ? (9 - kt) : MAXF;
    if (later == 0) wait_vm<0>();
    else if (later == 1) wait_vm<8>();
    else wait_vm<16>();
    bar();
    if (kt + NBUF - 1 < 10) issue(kt + NBUF - 1);
    const unsigned base = (kt % NBUF) * 32768;
#pragma unroll
    for (int ks = 0; ks < 2; ++ks) {
      bf16x8 a[2];
#pragma unroll
      for (int m = 0; m < 2; ++m) a[m] = lds_b128(smem, base + aoff[ks] + m * 2048);
#pragma unroll
      for (int n = 0; n < 8; ++n) {
        const bf16x8 bb = lds_tr2(smem, base + ks * 8192 + (boff[0] ^ (n << 5)), base + ks * 8192 + (boff[1] ^ (n << 5)));
#pragma unroll
        for (int m = 0; m < 2; ++m) acc[m][n] = n < 5 ? mfma16(a[m], bb, acc[m][n]) : mfma16(bb, a[m], acc[m][n]);
      }
    }
  }

#pragma unroll
  for (int m = 0; m < 2; ++m) {
    u16* dst = pq + (size_t)(rt * 128 + 32 * wv + 16 * m + c16) * DM + 40 * g + 4 * g4;
#pragma unroll
    for (int n = 5; n < 8; ++n) {
      u32x2 o;
      o[0] = pk2(phi(acc[m][n][0]), phi(acc[m][n][1]));
      o[1] = pk2(phi(acc[m][n][2]), phi(acc[m][n][3]));
      if (n < 7 || g4 < 2) *(u32x2*)(dst + 16 * (n - 5)) = o;
    }
  }
  bf16x8 fr[5];
  bf16x8 v2;
#pragma unroll
  for (int n = 0; n < 5; ++n) {
    unsigned u[4];
#pragma unroll
    for (int m = 0; m < 2; ++m)
#pragma unroll
      for (int j = 0; j < 2; ++j) {
        float a0 = acc[m][n][2 * j], a1 = acc[m][n][2 * j + 1];
        if (n < 2) { a0 = phi(a0); a1 = phi(a1); }
        if (n == 2) { a0 = c16 < 8 ? phi(a0) : a0; a1 = c16 < 8 ? phi(a1) : a1; }
        u[2 * m + j] = pk2(a0, a1);
      }
    fr[n] = mk8(u[0], u[1], u[2], u[3]);
    if (n == 2) {
      const unsigned one = 0x3f803f80u;
      v2 = c16 < 8 ? mk8(one, one, one, one) : fr[2];
    }
  }
  f32x4 dd[8];
  const f32x4 z4 = {0.f, 0.f, 0.f, 0.f};
  dd[0] = mfma16(fr[0], v2, z4);
  dd[1] = mfma16(fr[0], fr[3], z4);
  dd[2] = mfma16(fr[1], v2, z4);
  dd[3] = mfma16(fr[1], fr[3], z4);
  dd[4] = mfma16(fr[1], fr[4], z4);
  dd[5] = mfma16(fr[2], v2, z4);
  dd[6] = mfma16(fr[2], fr[3], z4);
  dd[7] = mfma16(fr[2], fr[4], z4);
  wait_vm<63>();
  bar();
#pragma unroll
  for (int p = 0; p < 8; ++p) *(LAS f32x4*)((LAS char*)smem + wv * 8192 + p * 1024 + lane * 16) = dd[p];
  asm volatile("s_waitcnt lgkmcnt(0)" ::: "memory");
  bar();
#pragma unroll
  for (int pp = 0; pp < 2; ++pp) {
    const int p = 2 * wv + pp;
    f32x4 s = *(const LAS f32x4*)((LAS char*)smem + p * 1024 + lane * 16);
#pragma unroll
    for (int w2 = 1; w2 < 4; ++w2) s += *(const LAS f32x4*)((LAS char*)smem + w2 * 8192 + p * 1024 + lane * 16);
    const int ta = p < 2 ? 0 : (p < 5 ? 1 : 2);
    const int tb = p < 2 ? 2 + p : (p < 5 ? p : p - 3);
    const int d = 16 * ta + 4 * g4;
    const int h = d >= 20;
    const int vc = 16 * tb + c16 - 40;
    int e;
    bool ok = d < 40;
    if (tb == 2 && c16 < 8) { e = 20; ok = ok && c16 == 0; }
    else { const int he = vc >= 20; e = vc - 20 * he; ok = ok && he == h; }
    if (ok) *(f32x4*)(part + ((size_t)(g * 16 + rt) * 2 + h) * 420 + e * 20 + (d - 20 * h)) = s;
  }
}

struct cf { float r, i; };
DEV cf cmul(cf a, cf b) { return {a.r * b.r - a.i * b.i, a.r * b.i + a.i * b.r}; }
DEV cf cexp_s(float a, float b, float s) {
  const float mag = __builtin_amdgcn_exp2f(a * (s * 1.4426950408889634f));
  const float rev = __builtin_amdgcn_fractf(b * (s * 0.15915494309189535f));
  return {mag * __builtin_amdgcn_cosf(rev), mag * __builtin_amdgcn_sinf(rev)};
}

DEV void kgen_wave(int d, const u16* __restrict__ Are, const u16* __restrict__ Aim, const u16* __restrict__ Bre,
                   const u16* __restrict__ Bim, const u16* __restrict__ Cre, const u16* __restrict__ Cim,
                   const u16* __restrict__ logdt, u16* __restrict__ R, char* smem, unsigned base) {
  const int lane = tidx() & 63;
  const float dt = rbf(exp_acc(bf2f(logdt[d])));
  u16 pa[3][6];
#pragma unroll
  for (int c = 0; c < 3; ++c) {
    const int idx = d * NM + c * 64 + lane;
    pa[c][0] = Are[idx]; pa[c][1] = Aim[idx]; pa[c][2] = Bre[idx];
    pa[c][3] = Bim[idx]; pa[c][4] = Cre[idx]; pa[c][5] = Cim[idx];
  }
  constexpr unsigned HI = 0, LO = 21 * 256;
  const unsigned wbase = base + 4 * lane;
  const int r32 = lane & 31, hh = lane >> 5;
  const int rhi = r32 > 20 ? 20 : r32;
  f32x16 acc;
#pragma unroll
  for (int i = 0; i < 16; ++i) acc[i] = 0.f;
#pragma unroll 1
  for (int c = 0; c < 3; ++c) {
    u16 p0, p1, p2, p3, p4, p5;
    if (c == 0) { p0 = pa[0][0]; p1 = pa[0][1]; p2 = pa[0][2]; p3 = pa[0][3]; p4 = pa[0][4]; p5 = pa[0][5]; }
    else if (c == 1) { p0 = pa[1][0]; p1 = pa[1][1]; p2 = pa[1][2]; p3 = pa[1][3]; p4 = pa[1][4]; p5 = pa[1][5]; }
    else { p0 = pa[2][0]; p1 = pa[2][1]; p2 = pa[2][2]; p3 = pa[2][3]; p4 = pa[2][4]; p5 = pa[2][5]; }
    const float a = dt * bf2f(p0), b = dt * bf2f(p1);
    const cf dB = {dt * bf2f(p2), dt * bf2f(p3)};
    const cf cc = cmul({bf2f(p4), bf2f(p5)}, dB);
    const cf w1 = cexp_s(a, b, 1.f), w8 = cexp_s(a, b, 8.f), w128 = cexp_s(a, b, 128.f), w256 = cexp_s(a, b, 256.f);
    const cf w2 = cmul(w1, w1), w4 = cmul(w2, w2);
    auto put = [&](unsigned tab, int r, float re, float im) LAM {
      *(LAS unsigned*)((LAS char*)smem + ((wbase - base) ^ (16u * (r & 15))) + base + tab + 256 * r) = pk2(re, im);
    };
    cf y = {1.f, 0.f}, y2 = w128;
#pragma unroll
    for (int j = 0; j < 16; ++j) {
      put(LO, j, y.r, y.i);
      put(LO, j + 16, y2.r, y2.i);
      if (j < 15) { y = j == 0 ? w8 : cmul(y, w8); y2 = cmul(y2, w8); }
    }
    cf A = cc;
    {
      cf t = A;
#pragma unroll
      for (int p = 0; p < 8; ++p) { put(HI, p, t.r, -t.i); if (p < 7) t = cmul(t, w1); }
    }
    A = cmul(A, w256);
    {
      cf t = A;
#pragma unroll
      for (int p = 0; p < 4; ++p) { put(HI, 8 + p, t.r, -t.i); if (p < 3) t = cmul(t, w2); }
    }
#pragma unroll
    for (int i = 0; i < 2; ++i) {
      A = cmul(A, w256);
      put(HI, 12 + 2 * i, A.r, -A.i);
      const cf t = cmul(A, w4);
      put(HI, 13 + 2 * i, t.r, -t.i);
    }
#pragma unroll
    for (int i = 0; i < 5; ++i) { A = cmul(A, w256); put(HI, 16 + i, A.r, -A.i); }
    asm volatile("s_waitcnt lgkmcnt(0)" ::: "memory");
#pragma unroll
    for (int ks = 0; ks < 8; ++ks) {
      const int ch = 2 * ks + hh;
      const bf16x8 fa = lds_b128(smem, base + HI + rhi * 256 + 16 * (ch ^ (rhi & 15)));
      const bf16x8 fb = lds_b128(smem, base + LO + r32 * 256 + 16 * (ch ^ (r32 & 15)));
      acc = mfma32(fa, fb, acc);
    }
    asm volatile("s_waitcnt lgkmcnt(0)" ::: "memory");
  }
  {
    const int j = r32;
    LAS char* T = (LAS char*)smem + base;
    u32x2 v;
    v[0] = pk2(acc[0], acc[1]); v[1] = pk2(acc[2], acc[3]);
    *(LAS u32x2*)(T + 2 * (8 * j + 4 * hh)) = v;
    v[0] = pk2(acc[4], acc[5]); v[1] = pk2(acc[6], acc[7]);
    if (hh == 0) *(LAS u32x2*)(T + 2 * (256 + 4 * j)) = v;
    else { *(LAS unsigned*)(T + 2 * (384 + 2 * j)) = v[0]; *(LAS unsigned*)(T + 2 * (448 + 2 * j)) = v[1]; }
    v[0] = pk2(acc[8], acc[9]); v[1] = pk2(acc[10], acc[11]);
    if (hh == 0) {
      *(LAS u16*)(T + 2 * (512 + j)) = (u16)v[0];
      *(LAS u16*)(T + 2 * (544 + j)) = (u16)(v[0] >> 16);
      *(LAS u16*)(T + 2 * (576 + j)) = (u16)v[1];
      *(LAS u16*)(T + 2 * (608 + j)) = (u16)(v[1] >> 16);
    } else if (j == 0) {
      *(LAS u16*)(T + 2 * 640) = (u16)v[0];
    }
    asm volatile("s_waitcnt lgkmcnt(0)" ::: "memory");
    u16* Rd = R + (size_t)d * RSTR;
    auto rd3 = [&](int idx, unsigned& a, unsigned& b, unsigned& c) LAM {
      const unsigned ab = *(const LAS unsigned*)(T + 2 * idx);
      a = ab & 0xffffu; b = ab >> 16;
      c = *(const LAS u16*)(T + 2 * (idx + 2));
    };
    unsigned a, b, c;
    rd3(512 + 2 * lane, a, b, c);
    {
      const unsigned aa = a | (a << 16), bb = b | (b << 16), c2 = c | (c << 16), ba = b | (a << 16);
      u32x4 s0 = {c2, c2, bb, bb}, s1 = {bb, ba, aa, aa};
      *(u32x4*)(Rd + 1008 - 16 * lane) = s0;
      *(u32x4*)(Rd + 1016 - 16 * lane) = s1;
    }
    rd3(384 + 2 * lane, a, b, c);
    {
      u32x4 s0 = {c | (c << 16), b | (b << 16), b | (a << 16), a | (a << 16)};
      *(u32x4*)(Rd + 1528 - 8 * lane) = s0;
    }
    rd3(256 + 2 * lane, a, b, c);
    {
      u32x2 s0 = {c | (b << 16), a | (a << 16)};
      *(u32x2*)(Rd + 1788 - 4 * lane) = s0;
    }
    {
      const u32x2 t = *(const LAS u32x2*)(T + 2 * (4 * lane));
      u32x2 s0 = {(t[1] >> 16) | (t[1] << 16), (t[0] >> 16) | (t[0] << 16)};
      *(u32x2*)(Rd + 2044 - 4 * lane) = s0;
    }
    if (lane < 16) *(unsigned*)(Rd + 2048 + 2 * lane) = 0u;
  }
}

DEV void s4prep_role(int j, const u16* __restrict__ x, const u16* Are, const u16* Aim, const u16* Bre, const u16* Bim,
                     const u16* Cre, const u16* Cim, const u16* logdt, const u16* __restrict__ gw,
                     u16* __restrict__ R, u16* __restrict__ uT, float* __restrict__ G, char* smem) {
  const int tid = tidx(), lane = tid & 63;
  const int wv = __builtin_amdgcn_readfirstlane(tid >> 6);
  const unsigned base = wv * 16384;
  {
    const int rt = j / 10, ct = j - rt * 10;
#pragma unroll
    for (int q = 0; q < 4; ++q) {
      const int row = 8 * q + (lane >> 3);
      const int ch = (lane & 7) ^ ((row >> 1) & 7);
      glds16(x + (size_t)(rt * 128 + 32 * wv + row) * DM + ct * 64 + ch * 8, smem, base + q * 1024);
    }
    for (int r = j * 4 + wv; r < SL; r += 640) {
      float s = 0.f;
#pragma unroll
      for (int it = 0; it < 2; ++it) {
        const int ch = lane + 64 * it;
        if (ch < 80) {
          const u32x4 xv = *(const u32x4*)(x + (size_t)r * DM + ch * 8);
          const u32x4 gv = *(const u32x4*)(gw + ch * 8);
#pragma unroll
          for (int e = 0; e < 4; ++e) {
            const unsigned pr = pk2(bflo(xv[e]) * bflo(gv[e]), bfhi(xv[e]) * bfhi(gv[e]));
            s += bflo(pr) + bfhi(pr);
          }
        }
      }
      s = xor_sum(s);
      s = rbf(s);
      const float gg = rbf(__builtin_amdgcn_rcpf(1.f + __builtin_amdgcn_exp2f(-1.4426950408889634f * s)));
      if (lane == 0) G[r] = gg;
    }
    wait_vm<0>();
    const int g4 = lane >> 4, q = (lane & 15) >> 2, p = lane & 3;
#pragma unroll
    for (int c = 0; c < 4; ++c) {
      unsigned off[2];
#pragma unroll
      for (int t = 0; t < 2; ++t) {
        const int row = 8 * g4 + 4 * t + q;
        off[t] = base + row * 128 + 16 * ((2 * c + (p >> 1)) ^ ((row >> 1) & 7)) + 8 * (p & 1);
      }
      const bf16x8 v = lds_tr2(smem, off[0], off[1]);
      *(bf16x8*)(uT + (size_t)(ct * 64 + 16 * c + (lane & 15)) * SL + rt * 128 + 32 * wv + 8 * g4) = v;
    }
    asm volatile("s_waitcnt lgkmcnt(0)" ::: "memory");
  }
  kgen_wave(j * 4 + wv, Are, Aim, Bre, Bim, Cre, Cim, logdt, R, smem, base);
}

constexpr int K1_NBUF = 2;
constexpr int K1_LDS = 65536;
NOPK __global__ __launch_bounds__(256, 2) void k1_kernel(const u16* __restrict__ x, const u16* __restrict__ wqkv,
                                                    const u16* Are, const u16* Aim, const u16* Bre, const u16* Bim,
                                                    const u16* Cre, const u16* Cim, const u16* logdt, const u16* gw,
                                                    char* __restrict__ ws) {
  extern __shared__ __attribute__((aligned(16))) char smem[];
  const int b = bidx();
  if (b < 256) qkv_role<K1_NBUF>(b, x, wqkv, (float*)(ws + WS_PART), (u16*)(ws + WS_PQ), smem);
  else s4prep_role(b - 256, x, Are, Aim, Bre, Bim, Cre, Cim, logdt, gw, (u16*)(ws + WS_R), (u16*)(ws + WS_UT),
                   (float*)(ws + WS_G), smem);
}

constexpr unsigned CV_CSTR = 4160;
constexpr unsigned CV_U = 4 * CV_CSTR;
constexpr unsigned CV_WAVE = CV_U + 512 + 4096 + 256;
DEV void conv_wave(int d, const u16* __restrict__ R, const u16* __restrict__ uT, const u16* __restrict__ Dskip,
                   u16* __restrict__ aT, char* smem, unsigned base) {
  const int lane = tidx() & 63;
#pragma unroll
  for (int q = 0; q < 4; ++q) glds16(uT + (size_t)d * SL + q * 512 + lane * 8, smem, base + CV_U + 512 + q * 1024);
  if (lane < 32) *(LAS u32x4*)((LAS char*)smem + base + CV_U + lane * 16) = (u32x4){0u, 0u, 0u, 0u};
  {
    const u16* Rd = R + (size_t)d * RSTR + 32 * lane;
    unsigned dw[18];
#pragma unroll
    for (int i = 0; i < 4; ++i) {
      const u32x4 v = *(const u32x4*)(Rd + 8 * i);
      dw[4 * i] = v[0]; dw[4 * i + 1] = v[1]; dw[4 * i + 2] = v[2]; dw[4 * i + 3] = v[3];
    }
    const u32x2 v2 = *(const u32x2*)(Rd + 32);
    dw[16] = v2[0]; dw[17] = v2[1];
    LAS char* W = (LAS char*)smem + base + 64 * lane;
#pragma unroll
    for (int i = 0; i < 4; ++i) {
      u32x4 c0, c1, c2, c3;
#pragma unroll
      for (int e = 0; e < 4; ++e) {
        const int k = 4 * i + e;
        c0[e] = dw[k];
        c1[e] = __builtin_amdgcn_alignbyte(dw[k + 1], dw[k], 2);
        c2[e] = dw[k + 1];
        c3[e] = __builtin_amdgcn_alignbyte(dw[k + 2], dw[k + 1], 2);
      }
      *(LAS u32x4*)(W + 16 * i) = c0;
      *(LAS u32x4*)(W + CV_CSTR + 16 * i) = c1;
      *(LAS u32x4*)(W + 2 * CV_CSTR + 16 * i) = c2;
      *(LAS u32x4*)(W + 3 * CV_CSTR + 16 * i) = c3;
    }
    if (lane < 8) *(LAS u32x4*)((LAS char*)smem + base + (lane >> 1) * CV_CSTR + 4096 + 16 * (lane & 1)) = (u32x4){0u, 0u, 0u, 0u};
  }
  wait_vm<0>();
  const int p = lane & 15, g4 = lane >> 4;
  const int m00 = 2031 + 8 * g4 - p;
  const unsigned kaddr = base + (m00 & 3) * CV_CSTR + 2 * (m00 & ~3);
  const unsigned uaddr = base + CV_U + 512 + 2 * (16 * p - 16 + 8 * g4);
  f32x4 acc[8];
#pragma unroll
  for (int t = 0; t < 8; ++t) acc[t] = (f32x4){0.f, 0.f, 0.f, 0.f};
#pragma unroll 1
  for (int r = 0; r < 8; ++r) {
    const unsigned ka = kaddr - 64 * r, ua = uaddr - 64 * r;
    bf16x8 uf[8];
#pragma unroll
    for (int i = 7; i >= 0; --i) {
      uf[7 - i] = lds_b128(smem, ua + 512 * (7 - i));
      const u32x2 k0 = *(const LAS u32x2*)((LAS char*)smem + (ka - 512 * i));
      const u32x2 k1 = *(const LAS u32x2*)((LAS char*)smem + (ka - 512 * i) + 8);
      const bf16x8 kf = mk8(k0[0], k0[1], k1[0], k1[1]);
#pragma unroll
      for (int t = i; t < 8; ++t) acc[t] = mfma16(kf, uf[t - i], acc[t]);
    }
  }
  const float dsk = bf2f(Dskip[d]);
#pragma unroll
  for (int t = 0; t < 8; ++t) {
    const int l0 = 256 * t + 16 * p + 4 * g4;
    const u32x2 uv = *(const LAS u32x2*)((LAS char*)smem + base + CV_U + 512 + 2 * l0);
    const unsigned y01 = pk2(acc[t][0], acc[t][1]), y23 = pk2(acc[t][2], acc[t][3]);
    const unsigned s01 = pk2(bflo(uv[0]) * dsk, bfhi(uv[0]) * dsk), s23 = pk2(bflo(uv[1]) * dsk, bfhi(uv[1]) * dsk);
    u32x2 o;
    o[0] = pk2(bflo(y01) + bflo(s01), bfhi(y01) + bfhi(s01));
    o[1] = pk2(bflo(y23) + bflo(s23), bfhi(y23) + bfhi(s23));
    *(u32x2*)(aT + (size_t)d * SL + l0) = o;
  }
}

DEV void norm_role(int j, const float* __restrict__ part, const u16* __restrict__ pq, u16* __restrict__ o, char* smem) {
  const int tid = tidx(), lane = tid & 63;
  const int wv = __builtin_amdgcn_readfirstlane(tid >> 6);
  const int g = j & 15, rq = j >> 4;
  LAS char* KV = (LAS char*)smem;
  for (int i = tid; i < 2 * 32 * 64 / 16; i += 256) *(LAS u32x4*)(KV + i * 16) = (u32x4){0u, 0u, 0u, 0u};
  asm volatile("s_waitcnt lgkmcnt(0)" ::: "memory"); bar();
  if (tid < 210) {
    const f32x4* src = (const f32x4*)(part + (size_t)g * 16 * 840) + tid;
    f32x4 s = src[0];
#pragma unroll
    for (int r = 1; r < 16; ++r) s += src[r * 210];
    const int h = tid / 105, rem = tid - h * 105, e = rem / 5, d0 = (rem - e * 5) * 4;
    u32x2 v = {pk2(s[0], s[1]), pk2(s[2], s[3])};
    *(LAS u32x2*)(KV + h * 2048 + e * 64 + d0 * 2) = v;
  }
  asm volatile("s_waitcnt lgkmcnt(0)" ::: "memory"); bar();
  const int c16 = lane & 15, g4 = lane >> 4;
  bf16x8 ka[2][2], kz[2];
#pragma unroll
  for (int h = 0; h < 2; ++h) {
    ka[h][0] = *(const LAS bf16x8*)(KV + h * 2048 + c16 * 64 + g4 * 16);
    ka[h][1] = *(const LAS bf16x8*)(KV + h * 2048 + (16 + (c16 & 3)) * 64 + g4 * 16);
    kz[h] = *(const LAS bf16x8*)(KV + h * 2048 + 20 * 64 + g4 * 16);
  }
  const f32x4 z4 = {0.f, 0.f, 0.f, 0.f};
#pragma unroll 2
  for (int it = 0; it < 8; ++it) {
    const int row = rq * 512 + wv * 128 + it * 16 + c16;
    const u16* src = pq + (size_t)row * DM + 40 * g;
    u16* dst = o + (size_t)row * DM + 40 * g;
#pragma unroll
    for (int h = 0; h < 2; ++h) {
      u32x4 b = {0u, 0u, 0u, 0u};
      if (g4 < 2) b = *(const u32x4*)(src + 20 * h + 8 * g4);
      else if (g4 == 2) { const u32x2 t = *(const u32x2*)(src + 20 * h + 16); b[0] = t[0]; b[1] = t[1]; }
      const bf16x8 bf = __builtin_bit_cast(bf16x8, b);
      const f32x4 n0 = mfma16(ka[h][0], bf, z4), n1 = mfma16(ka[h][1], bf, z4), dn = mfma16(kz[h], bf, z4);
      const float inv = __builtin_amdgcn_rcpf(rbf(rbf(dn[0]) + 1e-6f));
      u32x2 w0 = {pk2(rbf(n0[0]) * inv, rbf(n0[1]) * inv), pk2(rbf(n0[2]) * inv, rbf(n0[3]) * inv)};
      *(u32x2*)(dst + 20 * h + 4 * g4) = w0;
      if (g4 == 0) {
        u32x2 w1 = {pk2(rbf(n1[0]) * inv, rbf(n1[1]) * inv), pk2(rbf(n1[2]) * inv, rbf(n1[3]) * inv)};
        *(u32x2*)(dst + 20 * h + 16) = w1;
      }
    }
  }
}

constexpr int K2_LDS = 4 * CV_WAVE;
NOPK __global__ __launch_bounds__(256, 1) void k2_kernel(const u16* __restrict__ Dskip, char* __restrict__ ws) {
  extern __shared__ __attribute__((aligned(16))) char smem[];
  const int b = bidx();
  if (b < 160) {
    const int wv = __builtin_amdgcn_readfirstlane(tidx() >> 6);
    conv_wave(b * 4 + wv, (const u16*)(ws + WS_R), (const u16*)(ws + WS_UT), Dskip, (u16*)(ws + WS_AT), smem, wv * CV_WAVE);
  } else {
    norm_role(b - 160, (const float*)(ws + WS_PART), (const u16*)(ws + WS_PQ), (u16*)(ws + WS_O), smem);
  }
}

constexpr int K3_STAGE = 8192 + 10240;
constexpr int K3_NBUF = 4;
constexpr int K3_LDS = K3_STAGE * K3_NBUF;
NOPK __global__ __launch_bounds__(256, 1) void k3_kernel(const u16* __restrict__ wo, const char* __restrict__ ws,
                                                    u16* __restrict__ out) {
  extern __shared__ __attribute__((aligned(16))) char smem[];
  const u16* o = (const u16*)(ws + WS_O);
  const u16* aT = (const u16*)(ws + WS_AT);
  const float* G = (const float*)(ws + WS_G);
  const int tid = tidx(), lane = tid & 63;
  const int wv = __builtin_amdgcn_readfirstlane(tid >> 6);
  const int b = bidx(), xcd = b & 7, ii = b >> 3;
  const int rt = 4 * xcd + (ii >> 3), ct = ii & 7;

  const u16* osrc[2];
#pragma unroll
  for (int q = 0; q < 2; ++q) {
    const int row = 16 * wv + 8 * q + (lane >> 3);
    osrc[q] = o + (size_t)(rt * 64 + row) * DM + ((lane & 7) ^ ((row >> 1) & 7)) * 8;
  }
  const u16* wsrc[3];
#pragma unroll
  for (int q = 0; q < 3; ++q) {
    int ci = (wv + 4 * q) * 64 + lane;
    ci = ci < 640 ? ci : 639;
    const int rho = ci / 10, pos = ci - rho * 10;
    const int kr = (rho & ~12) | ((rho & 4) << 1) | ((rho & 8) >> 1);
    wsrc[q] = wo + (size_t)kr * DM + ct * 80 + pos * 8;
  }
  auto issue = [&](int kt) LAM {
    const unsigned base = (kt % K3_NBUF) * K3_STAGE;
#pragma unroll
    for (int q = 0; q < 2; ++q) glds16(osrc[q] + kt * 64, smem, base + wv * 2048 + q * 1024);
#pragma unroll
    for (int q = 0; q < 3; ++q)
      if (wv + 4 * q < 10) glds16(wsrc[q] + (size_t)kt * 64 * DM, smem, base + 8192 + (wv + 4 * q) * 1024);
  };
  const int c16 = lane & 15, g4 = lane >> 4;
  unsigned aoff[2];
  {
    const int row = 16 * wv + c16;
#pragma unroll
    for (int ks = 0; ks < 2; ++ks) aoff[ks] = row * 128 + 16 * ((4 * ks + g4) ^ ((row >> 1) & 7));
  }
  const unsigned boff = 8192 + (16 * (g4 >> 1) + 4 * (g4 & 1) + (c16 >> 2)) * 160 + (lane & 3) * 8;
  f32x4 acc[5];
#pragma unroll
  for (int n = 0; n < 5; ++n) acc[n] = (f32x4){0.f, 0.f, 0.f, 0.f};
  u32x2 av[5];
  const int l0 = rt * 64 + 16 * wv + 4 * g4;
#pragma unroll
  for (int n = 0; n < 5; ++n) av[n] = *(const u32x2*)(aT + (size_t)(ct * 80 + 16 * n + c16) * SL + l0);
  const f32x4 gv = *(const f32x4*)(G + l0);
  FENCE();
#pragma unroll
  for (int s = 0; s < K3_NBUF - 1; ++s) issue(s);

#pragma unroll
  for (int kt = 0; kt < 10; ++kt) {
    const int later = (9 - kt) < (K3_NBUF - 2) ? (9 - kt) : (K3_NBUF - 2);
    if (wv < 2) { if (later == 0) wait_vm<0>(); else if (later == 1) wait_vm<5>(); else wait_vm<10>(); }
    else { if (later == 0) wait_vm<0>(); else if (later == 1) wait_vm<4>(); else wait_vm<8>(); }
    bar();
    if (kt + K3_NBUF - 1 < 10) issue(kt + K3_NBUF - 1);
    const unsigned base = (kt % K3_NBUF) * K3_STAGE;
#pragma unroll
    for (int ks = 0; ks < 2; ++ks) {
      const bf16x8 a = lds_b128(smem, base + aoff[ks]);
#pragma unroll
      for (int n = 0; n < 5; ++n) {
        const bf16x8 bb = lds_tr2(smem, base + boff + ks * 5120 + n * 32, base + boff + ks * 5120 + 1280 + n * 32);
        acc[n] = mfma16(a, bb, acc[n]);
      }
    }
  }
  wait_vm<0>();
  bar();
  LAS char* T = (LAS char*)smem + wv * 4096;
#pragma unroll
  for (int n = 0; n < 5; ++n) {
    float r[4];
#pragma unroll
    for (int j = 0; j < 4; ++j) {
      const float a = j & 1 ? bfhi(av[n][j >> 1]) : bflo(av[n][j >> 1]);
      const float gg = gv[j];
      const float t0 = rbf(gg * a), t1 = rbf(rbf(1.f - gg) * rbf(acc[n][j]));
      r[j] = t0 + t1;
    }
    u32x2 v = {pk2(r[0], r[1]), pk2(r[2], r[3])};
    *(LAS u32x2*)(T + (16 * n + c16) * 32 + g4 * 8) = v;
  }
  asm volatile("s_waitcnt lgkmcnt(0)" ::: "memory");
  {
    const int q = c16 >> 2, p = lane & 3;
#pragma unroll
    for (int it = 0; it < 3; ++it) {
      int chn = g4 + 4 * it;
      const bool ok = chn < 10;
      chn = ok ? chn : 9;
      const unsigned o0 = wv * 4096 + (8 * chn + q) * 32 + p * 8;
      const bf16x8 v = lds_tr2(smem, o0, o0 + 4 * 32);
      if (ok) *(bf16x8*)(out + (size_t)(rt * 64 + 16 * wv + c16) * DM + ct * 80 + 8 * chn) = v;
    }
  }
}

extern "C" void kernel_launch(void* const* d_in, const int* in_sizes, int n_in, void* d_out, int out_size, void* d_ws,
                              size_t ws_size, hipStream_t stream) {
  const u16* x = (const u16*)d_in[0];
  const u16* Are = (const u16*)d_in[1];
  const u16* Aim = (const u16*)d_in[2];
  const u16* Bre = (const u16*)d_in[3];
  const u16* Bim = (const u16*)d_in[4];
  const u16* Cre = (const u16*)d_in[5];
  const u16* Cim = (const u16*)d_in[6];
  const u16* logdt = (const u16*)d_in[7];
  const u16* Dskip = (const u16*)d_in[8];
  const u16* wqkv = (const u16*)d_in[10];
  const u16* wo = (const u16*)d_in[11];
  const u16* gw = (const u16*)d_in[12];
  char* ws = (char*)d_ws;
  (void)hipFuncSetAttribute((const void*)k2_kernel, hipFuncAttributeMaxDynamicSharedMemorySize, K2_LDS);
  (void)hipFuncSetAttribute((const void*)k3_kernel, hipFuncAttributeMaxDynamicSharedMemorySize, K3_LDS);
  (void)hipFuncSetAttribute((const void*)k1_kernel, hipFuncAttributeMaxDynamicSharedMemorySize, K1_LDS);
  k1_kernel<<<256 + 160, 256, K1_LDS, stream>>>(x, wqkv, Are, Aim, Bre, Bim, Cre, Cim, logdt, gw, ws);
  k2_kernel<<<160 + 64, 256, K2_LDS, stream>>>(Dskip, ws);
  k3_kernel<<<256, 256, K3_LDS, stream>>>(wo, ws, (u16*)d_out);
}
```

```cpp
#include <hip/hip_runtime.h>
#include <hip/hip_bf16.h>
#include <stdint.h>

typedef unsigned short u16;
typedef unsigned long long u64;
typedef __attribute__((ext_vector_type(8))) __bf16 bf16x8;
typedef __attribute__((ext_vector_type(4))) __bf16 bf16x4;
typedef __attribute__((ext_vector_type(2))) __bf16 bf16x2;
typedef __attribute__((ext_vector_type(2))) float f32x2;
typedef __attribute__((ext_vector_type(4))) float f32x4;
typedef __attribute__((ext_vector_type(16))) float f32x16;
typedef __attribute__((ext_vector_type(2))) unsigned u32x2;
typedef __attribute__((ext_vector_type(4))) unsigned u32x4;
#define LAS __attribute__((address_space(3)))
#define GAS __attribute__((address_space(1)))
#define CAS __attribute__((address_space(4)))
#if defined(__HIP_DEVICE_COMPILE__)
#define NOPK __attribute__((target("no-packed-fp32-ops")))
#else
#define NOPK
#endif
#define DEV __device__ __forceinline__ NOPK
#define LAM __attribute__((always_inline)) NOPK

constexpr int SL = 2048;
constexpr int DM = 640;
constexpr int NM = 192;

constexpr size_t WS_EPOCH = 0;
constexpr size_t WS_TMO = 2048;
constexpr size_t WS_FPART = 4096;
constexpr size_t WS_FO = WS_FPART + 16384;
constexpr size_t WS_FU = WS_FO + 16384;
constexpr size_t WS_FA = WS_FU + 16384;
constexpr size_t WS_PART = 1u << 17;
constexpr size_t WS_O = 1u << 20;
constexpr size_t WS_UT = WS_O + (size_t)SL * DM * 2;
constexpr size_t WS_AT = WS_UT + (size_t)SL * DM * 2;
constexpr size_t WS_END = WS_AT + (size_t)SL * DM * 2;
constexpr int LDS_BYTES = 81920;

#define STAMP(i) do {} while (0)

DEV float bf2f(unsigned h) { return __builtin_bit_cast(float, h << 16); }
DEV float bflo(unsigned w) { return __builtin_bit_cast(float, w << 16); }
DEV float bfhi(unsigned w) { return __builtin_bit_cast(float, w & 0xffff0000u); }
DEV int tidx() { return __builtin_amdgcn_workitem_id_x(); }
DEV int bidx() { return __builtin_amdgcn_workgroup_id_x(); }
DEV unsigned pk2(float a, float b) {
  f32x2 v = {a, b};
  return __builtin_bit_cast(unsigned, __builtin_convertvector(v, bf16x2));
}
DEV float rbf(float a) { return bflo(pk2(a, a)); }
DEV bf16x8 mk8(unsigned a, unsigned b, unsigned c, unsigned d) {
  u32x4 v = {a, b, c, d};
  return __builtin_bit_cast(bf16x8, v);
}
DEV f32x4 mfma16(bf16x8 a, bf16x8 b, f32x4 c) { return __builtin_amdgcn_mfma_f32_16x16x32_bf16(a, b, c, 0, 0, 0); }
DEV f32x16 mfma32(bf16x8 a, bf16x8 b, f32x16 c) { return __builtin_amdgcn_mfma_f32_32x32x16_bf16(a, b, c, 0, 0, 0); }
DEV bf16x8 lds_b128(const char* smem, unsigned off) { return *(const LAS bf16x8*)((const LAS char*)smem + off); }
DEV u32x2 lds_b64(const char* smem, unsigned off) { return *(const LAS u32x2*)((const LAS char*)smem + off); }
DEV bf16x8 lds_tr2(const char* smem, unsigned off0, unsigned off1) {
  bf16x4 t0 = __builtin_amdgcn_ds_read_tr16_b64_v4bf16((LAS bf16x4*)((LAS char*)smem + off0));
  bf16x4 t1 = __builtin_amdgcn_ds_read_tr16_b64_v4bf16((LAS bf16x4*)((LAS char*)smem + off1));
  return __builtin_shufflevector(t0, t1, 0, 1, 2, 3, 4, 5, 6, 7);
}
DEV void glds_s(const void* sbase, unsigned voff, unsigned ldsaddr) {
  asm volatile("s_mov_b32 m0, %2\n\ts_nop 0\n\tglobal_load_lds_dwordx4 %0, %1" ::"v"(voff), "s"(sbase), "s"(ldsaddr) : "memory");
}
DEV void glds_v(const void* vaddr, unsigned ldsaddr) {
  asm volatile("s_mov_b32 m0, %1\n\ts_nop 0\n\tglobal_load_lds_dwordx4 %0, off" ::"v"(vaddr), "s"(ldsaddr) : "memory");
}
#define FENCE() asm volatile("" ::: "memory")
#define SCHED_BAR() __builtin_amdgcn_sched_barrier(0)
#define SGB(mask, n) __builtin_amdgcn_sched_group_barrier(mask, n, 0)
DEV void bar() { FENCE(); __builtin_amdgcn_s_barrier(); FENCE(); }
template <int N> DEV void wait_vm() { asm volatile("s_waitcnt vmcnt(%0) lgkmcnt(0)" ::"i"(N) : "memory"); }
DEV void wait_lgkm() { asm volatile("s_waitcnt lgkmcnt(0)" ::: "memory"); }
template <int N> struct IC { static constexpr int v = N; };
template <int I, int E, class F> DEV void sfor(F&& f) { if constexpr (I < E) { f(IC<I>{}); sfor<I + 1, E>(f); } }

DEV float exp_acc(float x) {
  const float hi = x * 1.4426950408889634f;
  const float lo = __builtin_fmaf(x, 1.4426950408889634f, -hi) + x * 1.9259629911e-8f;
  const float e = __builtin_amdgcn_exp2f(hi);
  return __builtin_fmaf(e, lo * 0.6931471805599453f, e);
}
DEV float phi(float t) {
  float e = __builtin_amdgcn_exp2f(t * 1.4426950408889634f);
  e = __builtin_amdgcn_fmed3f(e, 0.f, 1.f);
  return e + (__builtin_fmaxf(t, 0.f) + 1e-6f);
}

typedef __amdgpu_buffer_rsrc_t rsrc_t;
DEV rsrc_t mk_rsrc(const void* p, unsigned bytes) { return __builtin_amdgcn_make_buffer_rsrc((void*)p, 0, bytes, 0x00020000); }
constexpr int SC1 = 16;
DEV void st128(rsrc_t r, unsigned off, u32x4 v) { __builtin_amdgcn_raw_buffer_store_b128(v, r, off, 0, SC1); }
DEV void st64(rsrc_t r, unsigned off, u32x2 v) { __builtin_amdgcn_raw_buffer_store_b64(v, r, off, 0, SC1); }
DEV u32x4 ld128(rsrc_t r, unsigned off) { return __builtin_amdgcn_raw_buffer_load_b128(r, off, 0, SC1); }
DEV u32x2 ld64(rsrc_t r, unsigned off) { return __builtin_amdgcn_raw_buffer_load_b64(r, off, 0, SC1); }
DEV unsigned ld16(rsrc_t r, unsigned off) { return __builtin_amdgcn_raw_buffer_load_b16(r, off, 0, SC1); }
DEV void drain() { asm volatile("s_waitcnt vmcnt(0)" ::: "memory"); }
DEV void flag_set(char* ws, size_t off, u64 tag) {
  __hip_atomic_store((GAS u64*)(ws + off), tag, __ATOMIC_RELAXED, __HIP_MEMORY_SCOPE_AGENT);
}
DEV void flags_wait(char* ws, size_t off, int n, u64 tag) {
  const int lane = tidx() & 63;
  GAS u64* p = (GAS u64*)(ws + off) + (lane < n ? lane : 0) * 8;
  for (unsigned spins = 0; spins < (1u << 16); ++spins) {
    const u64 v = __hip_atomic_load(p, __ATOMIC_RELAXED, __HIP_MEMORY_SCOPE_AGENT);
    if (__builtin_amdgcn_ballot_w64(v != tag) == 0) { FENCE(); return; }
    __builtin_amdgcn_s_sleep(1);
  }
  if (lane == 0) __hip_atomic_store((GAS unsigned*)(ws + WS_TMO), 1u, __ATOMIC_RELAXED, __HIP_MEMORY_SCOPE_AGENT);
  FENCE();
}
DEV u64 read_tag(const char* ws) {
  u64 e;
  asm volatile("s_load_dwordx2 %0, %1, 0x0\n\ts_waitcnt lgkmcnt(0)" : "=s"(e) : "s"(ws) : "memory");
  return e + 1;
}

constexpr int wait_count(int s) {
  const int needW = s - 3, needX = 2 * (s >> 1) + 1 - 4;
  const int need = needW > needX ? needW : needX;
  int c = 0;
  for (int t = need + 1; t < s; ++t) { if (t + 4 >= 0 && 2 * (t + 4) < 40) c += 2; if (t + 3 >= 0 && 2 * (t + 3) < 40) c += 2; }
  return c;
}

DEV void attn_role(int b, const u16* __restrict__ x, const u16* __restrict__ w, const u16* __restrict__ wo,
                   const u16* __restrict__ gw, char* __restrict__ ws, u16* __restrict__ out, char* smem) {
  const int tid = tidx(), lane = tid & 63;
  const int wv = __builtin_amdgcn_readfirstlane(tid >> 6);
  const int xcd = b & 7, ii = b >> 3;
  const int rt = 4 * (xcd >> 1) + (ii >> 3), g = 8 * (xcd & 1) + (ii & 7);
  const int gq = g ^ 1;
  const int c16 = lane & 15, g4 = lane >> 4;
  const unsigned lds0 = (unsigned)(size_t)(LAS char*)smem;
  STAMP(0);

  constexpr unsigned WB = 3 * 16384;
  const u16* xb = x + (size_t)rt * 128 * DM;
  unsigned xo[4];
#pragma unroll
  for (int q = 0; q < 4; ++q) { const int row = 32 * wv + 8 * q + (lane >> 3); xo[q] = (row * DM + ((lane & 7) ^ ((row >> 1) & 7)) * 8) * 2; }
  const char* wp[2];
  int wstep[2];
#pragma unroll
  for (int q = 0; q < 2; ++q) {
    const int kr = 8 * wv + 4 * q + (lane >> 4);
    const int wc = (lane & 15) ^ (((kr & 3) << 2) | ((kr >> 2) & 3));
    const int seg = wc / 5;
    const int col = (seg == 0 ? DM + 40 * g : (seg == 1 ? 2 * DM + 40 * g : 40 * gq)) + (wc - seg * 5) * 8;
    const bool isg = wc == 15;
    wp[q] = isg ? (const char*)(gw + (kr & ~7)) : (const char*)(w + (size_t)kr * (3 * DM) + col);
    wstep[q] = isg ? 64 : 32 * 3 * DM * 2;
  }
  auto xissue = [&](int p) LAM { if (p < 40) glds_s(xb + (p >> 2) * 64, xo[p & 3], lds0 + ((p >> 2) % 3) * 16384 + wv * 4096 + (p & 3) * 1024); };
  auto wissue = [&](int p) LAM {
    if (p < 40) {
      glds_v(wp[p & 1], lds0 + WB + ((p >> 1) % 4) * 8192 + (2 * wv + (p & 1)) * 1024);
      wp[p & 1] += wstep[p & 1];
    }
  };
  unsigned aoff[2];
  { const int row = 32 * wv + c16;
#pragma unroll
    for (int ks = 0; ks < 2; ++ks) aoff[ks] = row * 128 + 16 * ((4 * ks + g4) ^ ((row >> 1) & 7)); }
  unsigned boff[2][8];
  { const int q = c16 >> 2, p = lane & 3;
#pragma unroll
    for (int t = 0; t < 2; ++t) { const int kr = 8 * g4 + 4 * t + q; const int f = ((kr & 3) << 2) | ((kr >> 2) & 3);
#pragma unroll
      for (int n = 0; n < 8; ++n) boff[t][n] = (WB + kr * 256 + 16 * ((p >> 1) ^ f) + 8 * (p & 1)) ^ (n << 5); } }
  unsigned gmask[4];
#pragma unroll
  for (int r = 0; r < 4; ++r) gmask[r] = c16 < 8 ? 0xffffffffu : (c16 - 8 == 2 * r ? 0x0000ffffu : (c16 - 8 == 2 * r + 1 ? 0xffff0000u : 0u));

  sfor<0, 4>([&](auto S) LAM { constexpr int s = S.v - 4;
    xissue(2 * (s + 4)); xissue(2 * (s + 4) + 1);
    if constexpr (s + 3 >= 0) { wissue(2 * (s + 3)); wissue(2 * (s + 3) + 1); } });
  const u64 tag = read_tag(ws);

  f32x4 acc[2][8];
#pragma unroll
  for (int m = 0; m < 2; ++m)
#pragma unroll
    for (int n = 0; n < 8; ++n) acc[m][n] = (f32x4){0.f, 0.f, 0.f, 0.f};
  bf16x8 fa[2][2], fb[2][8];
  auto rdA = [&](int par, int s) LAM {
#pragma unroll
    for (int m = 0; m < 2; ++m) fa[par][m] = lds_b128(smem, ((s >> 1) % 3) * 16384 + aoff[s & 1] + m * 2048);
  };
  auto rdB = [&](int par, int s, int n) LAM {
    fb[par][n] = lds_tr2(smem, (s % 4) * 8192 + boff[0][n], (s % 4) * 8192 + boff[1][n]);
  };
  auto mm = [&](int par, int n, int s) LAM {
    if (n == 7) {
      u32x4 v = __builtin_bit_cast(u32x4, fb[par][7]);
      v[0] &= gmask[0]; v[1] &= gmask[1]; v[2] &= gmask[2]; v[3] &= gmask[3];
      fb[par][7] = __builtin_bit_cast(bf16x8, v);
    }
#pragma unroll
    for (int m = 0; m < 2; ++m) acc[m][n] = n < 5 ? mfma16(fa[par][m], fb[par][n], acc[m][n]) : mfma16(fb[par][n], fa[par][m], acc[m][n]);
  };
#define GRP3() SGB(0x8, 1); SGB(0x100, 2); SGB(0x8, 1); SGB(0x100, 2); SGB(0x8, 1); SGB(0x100, 2); SGB(0x8, 1)
  sfor<0, 20>([&](auto S) LAM {
    constexpr int s = S.v;
    wait_vm<wait_count(s)>();
    bar();
    if (s == 0) STAMP(1);
    constexpr int cur = s & 1, prv = cur ^ 1;
    rdA(cur, s); rdB(cur, s, 0); rdB(cur, s, 1);
    if constexpr (s > 0) { mm(prv, 0, s - 1); mm(prv, 1, s - 1); GRP3(); }
    SCHED_BAR();
    wissue(2 * (s + 3));
    SCHED_BAR();
    rdB(cur, s, 2); rdB(cur, s, 3); rdB(cur, s, 4);
    if constexpr (s > 0) { mm(prv, 2, s - 1); mm(prv, 3, s - 1); GRP3(); }
    SCHED_BAR();
    wissue(2 * (s + 3) + 1);
    SCHED_BAR();
    rdB(cur, s, 5); rdB(cur, s, 6); rdB(cur, s, 7);
    if constexpr (s > 0) { mm(prv, 4, s - 1); mm(prv, 5, s - 1); GRP3(); }
    SCHED_BAR();
    xissue(2 * (s + 4));
    SCHED_BAR();
    if constexpr (s > 0) mm(prv, 6, s - 1);
    SCHED_BAR();
    xissue(2 * (s + 4) + 1);
    SCHED_BAR();
    if constexpr (s > 0) mm(prv, 7, s - 1);
    SCHED_BAR();
  });
#pragma unroll
  for (int n = 0; n < 8; ++n) mm(1, n, 19);
  STAMP(2);

  f32x4 dd[8];
  {
    bf16x8 fr[5], v2;
#pragma unroll
    for (int n = 0; n < 5; ++n) {
      unsigned u[4];
#pragma unroll
      for (int m = 0; m < 2; ++m)
#pragma unroll
        for (int j = 0; j < 2; ++j) {
          float a0 = acc[m][n][2 * j], a1 = acc[m][n][2 * j + 1];
          if (n < 2) { a0 = phi(a0); a1 = phi(a1); }
          if (n == 2) { a0 = c16 < 8 ? phi(a0) : a0; a1 = c16 < 8 ? phi(a1) : a1; }
          u[2 * m + j] = pk2(a0, a1);
        }
      fr[n] = mk8(u[0], u[1], u[2], u[3]);
      if (n == 2) { const unsigned one = 0x3f803f80u; v2 = c16 < 8 ? mk8(one, one, one, one) : fr[2]; }
    }
    const f32x4 z4 = {0.f, 0.f, 0.f, 0.f};
    dd[0] = mfma16(fr[0], v2, z4);
    dd[1] = mfma16(fr[0], fr[3], z4);
    dd[2] = mfma16(fr[1], v2, z4);
    dd[3] = mfma16(fr[1], fr[3], z4);
    dd[4] = mfma16(fr[1], fr[4], z4);
    dd[5] = mfma16(fr[2], v2, z4);
    dd[6] = mfma16(fr[2], fr[3], z4);
    dd[7] = mfma16(fr[2], fr[4], z4);
  }
  wait_lgkm();
  bar();
  const u16* wob = wo + 80 * (g & 7);
  auto wo_off = [&](int ci) LAM { const int rho = ci / 10, pos = ci - rho * 10; const int kr = (rho & ~12) | ((rho & 4) << 1) | ((rho & 8) >> 1); return (unsigned)((kr * DM + pos * 8) * 2); };
  unsigned woff5[5];
#pragma unroll
  for (int i = 0; i < 5; ++i) woff5[i] = wo_off(64 * i + lane);
  auto wo_issue = [&](int step, unsigned dst) LAM {
#pragma unroll
    for (int i = 0; i < 5; ++i)
      if (((step * 5 + i) & 3) == wv) glds_s(wob + (size_t)step * 32 * DM, woff5[i], lds0 + dst + i * 1024);
  };
#pragma unroll
  for (int p = 0; p < 8; ++p) *(LAS f32x4*)((LAS char*)smem + 49152 + wv * 8192 + p * 1024 + lane * 16) = dd[p];
  float gate_l[2];
#pragma unroll
  for (int m = 0; m < 2; ++m) {
    const float part0 = (acc[m][7][0] + acc[m][7][1]) + (acc[m][7][2] + acc[m][7][3]);
    float sum = part0;
    sum += __builtin_bit_cast(float, __builtin_amdgcn_ds_bpermute((lane ^ 16) << 2, __builtin_bit_cast(int, part0)));
    gate_l[m] = sum;
  }
  wait_lgkm();
  bar();
  {
    const rsrc_t rp = mk_rsrc(ws + WS_PART, 16 * 16 * 3360);
#pragma unroll
    for (int pp = 0; pp < 2; ++pp) {
      const int p = 2 * wv + pp;
      f32x4 s = *(const LAS f32x4*)((LAS char*)smem + 49152 + p * 1024 + lane * 16);
#pragma unroll
      for (int w2 = 1; w2 < 4; ++w2) s += *(const LAS f32x4*)((LAS char*)smem + 49152 + w2 * 8192 + p * 1024 + lane * 16);
      const int ta = p < 2 ? 0 : (p < 5 ? 1 : 2);
      const int tb = p < 2 ? 2 + p : (p < 5 ? p : p - 3);
      const int d = 16 * ta + 4 * g4;
      const int h = d >= 20;
      const int vc = 16 * tb + c16 - 40;
      int e;
      bool ok = d < 40;
      if (tb == 2 && c16 < 8) { e = 20; ok = ok && c16 == 0; }
      else { const int he = vc >= 20; e = vc - 20 * he; ok = ok && he == h; }
      if (ok) st128(rp, (((g * 16 + rt) * 2 + h) * 420 + e * 20 + (d - 20 * h)) * 4, __builtin_bit_cast(u32x4, s));
    }
  }
  drain();
  bar();
  if (tid == 0) flag_set(ws, WS_FPART + (g * 16 + rt) * 64, tag);
  STAMP(3);

#pragma unroll
  for (int i = 0; i < 11; ++i) wo_issue(i, 5120 * i);
#pragma unroll
  for (int i = 0; i < 4; ++i) wo_issue(16 + i, 5120 * (11 + i));
  bf16x8 p01[2], p2[2];
#pragma unroll
  for (int m = 0; m < 2; ++m) {
    p01[m] = mk8(pk2(phi(acc[m][5][0]), phi(acc[m][5][1])), pk2(phi(acc[m][5][2]), phi(acc[m][5][3])),
                 pk2(phi(acc[m][6][0]), phi(acc[m][6][1])), pk2(phi(acc[m][6][2]), phi(acc[m][6][3])));
    const unsigned a = pk2(phi(acc[m][7][0]), phi(acc[m][7][1])), c = pk2(phi(acc[m][7][2]), phi(acc[m][7][3]));
    p2[m] = mk8(g4 < 2 ? a : 0u, g4 < 2 ? c : 0u, 0u, 0u);
  }
  {
    LAS float* gl = (LAS float*)((LAS char*)smem + 76800 + 4096);
#pragma unroll
    for (int m = 0; m < 2; ++m) {
      const float s = rbf(gate_l[m]);
      const float gg = rbf(__builtin_amdgcn_rcpf(1.f + __builtin_amdgcn_exp2f(-1.4426950408889634f * s)));
      if (g4 == 2) gl[32 * wv + 16 * m + c16] = gg;
    }
    *(LAS u32x4*)((LAS char*)smem + 76800 + tid * 16) = (u32x4){0u, 0u, 0u, 0u};
  }
  wait_vm<0>();
  bar();
  const unsigned boff3 = (16 * (g4 >> 1) + 4 * (g4 & 1) + (c16 >> 2)) * 160 + (lane & 3) * 8;
  bf16x8 wr[4][5];
#pragma unroll
  for (int i = 0; i < 4; ++i)
#pragma unroll
    for (int n = 0; n < 5; ++n) wr[i][n] = lds_tr2(smem, 5120 * (11 + i) + boff3 + n * 32, 5120 * (11 + i) + boff3 + 1280 + n * 32);
  const int half = g >> 3;
  const float gate = *(const LAS float*)((LAS char*)smem + 76800 + 4096 + (64 * half + 16 * wv + c16) * 4);
  wait_lgkm();
  bar();
#pragma unroll
  for (int i = 11; i < 15; ++i) wo_issue(i, 5120 * i);

  STAMP(4);
  flags_wait(ws, WS_FPART + gq * 16 * 64, 16, tag);
  STAMP(5);
  {
    if (tid < 210) {
      const rsrc_t rp = mk_rsrc(ws + WS_PART + (size_t)gq * 16 * 3360, 16 * 3360);
      u32x4 v[16];
#pragma unroll
      for (int r = 0; r < 16; ++r) v[r] = ld128(rp, r * 3360 + tid * 16);
      f32x4 s = __builtin_bit_cast(f32x4, v[0]);
#pragma unroll
      for (int r = 1; r < 16; ++r) s += __builtin_bit_cast(f32x4, v[r]);
      const int h = tid / 105, rem = tid - h * 105, e = rem / 5, d0 = (rem - e * 5) * 4;
      const int row = e < 20 ? 20 * h + e : 40 + h;
      u32x2 pv = {pk2(s[0], s[1]), pk2(s[2], s[3])};
      *(LAS u32x2*)((LAS char*)smem + 76800 + row * 96 + (20 * h + d0) * 2) = pv;
    }
    wait_lgkm();
    bar();
    const f32x4 z4 = {0.f, 0.f, 0.f, 0.f};
    auto afr = [&](int row, bf16x8& a01, bf16x8& a2) LAM {
      const unsigned o = 76800 + row * 96 + 8 * g4;
      const u32x2 t0 = lds_b64(smem, o), t1 = lds_b64(smem, o + 32), t2 = lds_b64(smem, o + 64);
      a01 = mk8(t0[0], t0[1], t1[0], t1[1]);
      a2 = mk8(g4 < 2 ? t2[0] : 0u, g4 < 2 ? t2[1] : 0u, 0u, 0u);
    };
    bf16x8 k01[3], k2[3], z01[2], z2[2];
#pragma unroll
    for (int T = 0; T < 3; ++T) afr(T < 2 ? 16 * T + c16 : 32 + (c16 & 7), k01[T], k2[T]);
    afr(40, z01[0], z2[0]);
    afr(41, z01[1], z2[1]);
    u32x2 ov[2][3];
#pragma unroll
    for (int m = 0; m < 2; ++m) {
      const f32x4 d0 = mfma16(z01[0], p01[m], z4);
      const f32x4 d1 = mfma16(z2[1], p2[m], mfma16(z01[1], p01[m], z4));
      const float inv0 = __builtin_amdgcn_rcpf(rbf(rbf(d0[0]) + 1e-6f)), inv1 = __builtin_amdgcn_rcpf(rbf(rbf(d1[0]) + 1e-6f));
#pragma unroll
      for (int T = 0; T < 3; ++T) {
        const f32x4 nn = mfma16(k2[T], p2[m], mfma16(k01[T], p01[m], z4));
        const float inv = T == 0 ? inv0 : (T == 2 ? inv1 : (g4 == 0 ? inv0 : inv1));
        ov[m][T][0] = pk2(rbf(nn[0]) * inv, rbf(nn[1]) * inv);
        ov[m][T][1] = pk2(rbf(nn[2]) * inv, rbf(nn[3]) * inv);
      }
    }
    const rsrc_t ro = mk_rsrc(ws + WS_O, SL * DM * 2);
#pragma unroll
    for (int T = 0; T < 3; ++T) {
      const auto s0 = __builtin_amdgcn_permlane16_swap(ov[0][T][0], ov[1][T][0], false, false);
      const auto s1 = __builtin_amdgcn_permlane16_swap(ov[0][T][1], ov[1][T][1], false, false);
      const u32x4 v = {s0[0], s1[0], s0[1], s1[1]};
      const int c8 = 5 * gq + 2 * T + (g4 >> 1);
      const int rowt = 8 * rt + 2 * wv + (g4 & 1);
      if (T < 2 || g4 < 2) st128(ro, ((rowt * 20 + (c8 >> 2)) * 64 + (c8 & 3) * 16 + c16) * 16, v);
    }
  }
  drain();
  bar();
  if (tid == 0) flag_set(ws, WS_FO + (rt * 16 + gq) * 64, tag);
  STAMP(6);
  wo_issue(15, 5120 * 15);

  const int ct = g & 7;
  const int row0 = 128 * rt + 64 * half + 16 * wv;
  flags_wait(ws, WS_FA + ct * 32 * 64, 32, tag);
  STAMP(7);
  unsigned av[5][4];
  {
    const rsrc_t ra = mk_rsrc(ws + WS_AT, SL * DM * 2);
#pragma unroll
    for (int n = 0; n < 5; ++n)
#pragma unroll
      for (int j = 0; j < 4; ++j) av[n][j] = ld16(ra, ((80 * ct + 16 * n + 4 * g4 + j) * SL + row0 + c16) * 2);
  }
  u64 fa_chk[4];
  flags_wait(ws, WS_FO + rt * 16 * 64, 16, tag);
  STAMP(8);
  u32x4 of[20];
  {
    const rsrc_t ro = mk_rsrc(ws + WS_O, SL * DM * 2);
#pragma unroll
    for (int s = 0; s < 20; ++s) of[s] = ld128(ro, (((row0 >> 4) * 20 + s) * 64 + lane) * 16);
  }
  if (b == 0 && wv == 0) {
#pragma unroll
    for (int i = 0; i < 4; ++i) fa_chk[i] = __hip_atomic_load((GAS u64*)(ws + WS_FA) + (64 * i + lane) * 8, __ATOMIC_RELAXED, __HIP_MEMORY_SCOPE_AGENT);
  }
  asm volatile("s_waitcnt vmcnt(0)" ::: "memory");
  bar();
  STAMP(9);
  f32x4 oc[5];
#pragma unroll
  for (int n = 0; n < 5; ++n) oc[n] = (f32x4){0.f, 0.f, 0.f, 0.f};
#pragma unroll
  for (int s = 0; s < 20; ++s) {
    const bf16x8 bo = __builtin_bit_cast(bf16x8, of[s]);
#pragma unroll
    for (int n = 0; n < 5; ++n) {
      const bf16x8 wf = s < 16 ? lds_tr2(smem, 5120 * s + boff3 + n * 32, 5120 * s + boff3 + 1280 + n * 32) : wr[s < 16 ? 0 : s - 16][n];
      oc[n] = mfma16(wf, bo, oc[n]);
    }
  }
  STAMP(10);
  {
    const float g1 = rbf(1.f - gate);
    u32x2 r[5];
#pragma unroll
    for (int n = 0; n < 5; ++n) {
      float t[4];
#pragma unroll
      for (int j = 0; j < 4; ++j) t[j] = rbf(gate * bf2f(av[n][j])) + rbf(g1 * rbf(oc[n][j]));
      r[n][0] = pk2(t[0], t[1]); r[n][1] = pk2(t[2], t[3]);
    }
    u16* dst = out + (size_t)(row0 + c16) * DM + 80 * ct;
#pragma unroll
    for (int n = 0; n < 4; n += 2) {
      const auto s0 = __builtin_amdgcn_permlane16_swap(r[n][0], r[n + 1][0], false, false);
      const auto s1 = __builtin_amdgcn_permlane16_swap(r[n][1], r[n + 1][1], false, false);
      const u32x4 v = {s0[0], s1[0], s0[1], s1[1]};
      *(u32x4*)(dst + 16 * (n + (g4 & 1)) + 8 * (g4 >> 1)) = v;
    }
    *(u32x2*)(dst + 64 + 4 * g4) = r[4];
  }
  STAMP(11);
  if (b == 0 && wv == 0) {
    bool all = true;
#pragma unroll
    for (int i = 0; i < 4; ++i) all = all && fa_chk[i] == tag;
    if (__builtin_amdgcn_ballot_w64(!all) != 0) {
#pragma unroll 1
      for (int i = 0; i < 4; ++i) flags_wait(ws, WS_FA + i * 64 * 64, 64, tag);
    }
    if (lane == 0) __hip_atomic_store((GAS u64*)(ws + WS_EPOCH), tag, __ATOMIC_RELAXED, __HIP_MEMORY_SCOPE_AGENT);
  }
}

struct cf { float r, i; };
DEV cf cmul(cf a, cf b) { return {a.r * b.r - a.i * b.i, a.r * b.i + a.i * b.r}; }
DEV cf cexp_s(float a, float b, float s) {
  const float mag = __builtin_amdgcn_exp2f(a * (s * 1.4426950408889634f));
  const float rev = __builtin_amdgcn_fractf(b * (s * 0.15915494309189535f));
  return {mag * __builtin_amdgcn_cosf(rev), mag * __builtin_amdgcn_sinf(rev)};
}

constexpr unsigned CV_CSTR = 4160;
constexpr unsigned CV_U = 4 * CV_CSTR;
constexpr unsigned CV_WAVE = CV_U + 512 + 4096 + 256;

DEV void s4_wave(int d, int cg, const u16* __restrict__ Are, const u16* __restrict__ Aim, const u16* __restrict__ Bre,
                 const u16* __restrict__ Bim, const u16* __restrict__ Cre, const u16* __restrict__ Cim,
                 const u16* __restrict__ logdt, const u16* __restrict__ Dskip, char* __restrict__ ws, u64 tag,
                 char* smem, unsigned base) {
  const int lane = tidx() & 63;
  u16 pa[3][6];
#pragma unroll
  for (int c = 0; c < 3; ++c) {
    const int idx = d * NM + c * 64 + lane;
    pa[c][0] = Are[idx]; pa[c][1] = Aim[idx]; pa[c][2] = Bre[idx];
    pa[c][3] = Bim[idx]; pa[c][4] = Cre[idx]; pa[c][5] = Cim[idx];
  }
  const float dt = rbf(exp_acc(bf2f(logdt[d])));
  const float dsk = bf2f(Dskip[d]);
  constexpr unsigned HI = 0, LO = 21 * 256;
  const int r32 = lane & 31, hh = lane >> 5;
  const int rhi = r32 > 20 ? 20 : r32;
  f32x16 acc;
#pragma unroll
  for (int i = 0; i < 16; ++i) acc[i] = 0.f;
#pragma unroll 1
  for (int c = 0; c < 3; ++c) {
    u16 p0, p1, p2, p3, p4, p5;
    if (c == 0) { p0 = pa[0][0]; p1 = pa[0][1]; p2 = pa[0][2]; p3 = pa[0][3]; p4 = pa[0][4]; p5 = pa[0][5]; }
    else if (c == 1) { p0 = pa[1][0]; p1 = pa[1][1]; p2 = pa[1][2]; p3 = pa[1][3]; p4 = pa[1][4]; p5 = pa[1][5]; }
    else { p0 = pa[2][0]; p1 = pa[2][1]; p2 = pa[2][2]; p3 = pa[2][3]; p4 = pa[2][4]; p5 = pa[2][5]; }
    const float a = dt * bf2f(p0), b = dt * bf2f(p1);
    const cf dB = {dt * bf2f(p2), dt * bf2f(p3)};
    const cf cc = cmul({bf2f(p4), bf2f(p5)}, dB);
    const cf w1 = cexp_s(a, b, 1.f), w8 = cexp_s(a, b, 8.f), w128 = cexp_s(a, b, 128.f), w256 = cexp_s(a, b, 256.f);
    const cf w2 = cmul(w1, w1), w4 = cmul(w2, w2);
    auto put = [&](unsigned tab, int r, float re, float im) LAM {
      *(LAS unsigned*)((LAS char*)smem + ((4u * lane) ^ (16u * (r & 15))) + base + tab + 256 * r) = pk2(re, im);
    };
    cf y = {1.f, 0.f}, y2 = w128;
#pragma unroll
    for (int j = 0; j < 16; ++j) {
      put(LO, j, y.r, y.i);
      put(LO, j + 16, y2.r, y2.i);
      if (j < 15) { y = j == 0 ? w8 : cmul(y, w8); y2 = cmul(y2, w8); }
    }
    cf A = cc;
    {
      cf t = A;
#pragma unroll
      for (int p = 0; p < 8; ++p) { put(HI, p, t.r, -t.i); if (p < 7) t = cmul(t, w1); }
    }
    A = cmul(A, w256);
    {
      cf t = A;
#pragma unroll
      for (int p = 0; p < 4; ++p) { put(HI, 8 + p, t.r, -t.i); if (p < 3) t = cmul(t, w2); }
    }
#pragma unroll
    for (int i = 0; i < 2; ++i) {
      A = cmul(A, w256);
      put(HI, 12 + 2 * i, A.r, -A.i);
      const cf t = cmul(A, w4);
      put(HI, 13 + 2 * i, t.r, -t.i);
    }
#pragma unroll
    for (int i = 0; i < 5; ++i) { A = cmul(A, w256); put(HI, 16 + i, A.r, -A.i); }
    wait_lgkm();
#pragma unroll
    for (int ks = 0; ks < 8; ++ks) {
      const int ch = 2 * ks + hh;
      const bf16x8 fa = lds_b128(smem, base + HI + rhi * 256 + 16 * (ch ^ (rhi & 15)));
      const bf16x8 fb = lds_b128(smem, base + LO + r32 * 256 + 16 * (ch ^ (r32 & 15)));
      acc = mfma32(fa, fb, acc);
    }
    wait_lgkm();
  }
  STAMP(2);
  flags_wait(ws, WS_FU + cg * 32 * 64, 32, tag);
  u32x4 uv[4];
  {
    const rsrc_t ru = mk_rsrc(ws + WS_UT, SL * DM * 2);
#pragma unroll
    for (int q = 0; q < 4; ++q) uv[q] = ld128(ru, (d * SL + q * 512 + lane * 8) * 2);
  }
  STAMP(3);
  LAS char* W0 = (LAS char*)smem + base;
  {
    const int j = r32;
    LAS char* T = W0 + 3 * CV_CSTR;
    u32x2 v;
    v[0] = pk2(acc[0], acc[1]); v[1] = pk2(acc[2], acc[3]);
    *(LAS u32x2*)(T + 2 * (8 * j + 4 * hh)) = v;
    v[0] = pk2(acc[4], acc[5]); v[1] = pk2(acc[6], acc[7]);
    if (hh == 0) *(LAS u32x2*)(T + 2 * (256 + 4 * j)) = v;
    else { *(LAS unsigned*)(T + 2 * (384 + 2 * j)) = v[0]; *(LAS unsigned*)(T + 2 * (448 + 2 * j)) = v[1]; }
    v[0] = pk2(acc[8], acc[9]); v[1] = pk2(acc[10], acc[11]);
    if (hh == 0) {
      *(LAS u16*)(T + 2 * (512 + j)) = (u16)v[0];
      *(LAS u16*)(T + 2 * (544 + j)) = (u16)(v[0] >> 16);
      *(LAS u16*)(T + 2 * (576 + j)) = (u16)v[1];
      *(LAS u16*)(T + 2 * (608 + j)) = (u16)(v[1] >> 16);
    } else if (j == 0) {
      *(LAS u16*)(T + 2 * 640) = (u16)v[0];
    }
    wait_lgkm();
    auto rd3 = [&](int idx, unsigned& a, unsigned& b, unsigned& c) LAM {
      const unsigned ab = *(const LAS unsigned*)(T + 2 * idx);
      a = ab & 0xffffu; b = ab >> 16;
      c = *(const LAS u16*)(T + 2 * (idx + 2));
    };
    unsigned a, b, c;
    rd3(512 + 2 * lane, a, b, c);
    {
      const unsigned aa = a | (a << 16), bb = b | (b << 16), c2 = c | (c << 16), ba = b | (a << 16);
      *(LAS u32x4*)(W0 + 2 * (1008 - 16 * lane)) = (u32x4){c2, c2, bb, bb};
      *(LAS u32x4*)(W0 + 2 * (1016 - 16 * lane)) = (u32x4){bb, ba, aa, aa};
    }
    rd3(384 + 2 * lane, a, b, c);
    *(LAS u32x4*)(W0 + 2 * (1528 - 8 * lane)) = (u32x4){c | (c << 16), b | (b << 16), b | (a << 16), a | (a << 16)};
    rd3(256 + 2 * lane, a, b, c);
    *(LAS u32x2*)(W0 + 2 * (1788 - 4 * lane)) = (u32x2){c | (b << 16), a | (a << 16)};
    const u32x2 t = *(const LAS u32x2*)(T + 2 * (4 * lane));
    *(LAS u32x2*)(W0 + 2 * (2044 - 4 * lane)) = (u32x2){(t[1] >> 16) | (t[1] << 16), (t[0] >> 16) | (t[0] << 16)};
    if (lane < 2) *(LAS u32x4*)(W0 + 4096 + 16 * lane) = (u32x4){0u, 0u, 0u, 0u};
    wait_lgkm();
    unsigned dw[18];
#pragma unroll
    for (int i = 0; i < 4; ++i) {
      const u32x4 q = *(const LAS u32x4*)(W0 + 64 * lane + 16 * i);
      dw[4 * i] = q[0]; dw[4 * i + 1] = q[1]; dw[4 * i + 2] = q[2]; dw[4 * i + 3] = q[3];
    }
    const u32x2 q2 = *(const LAS u32x2*)(W0 + 64 * lane + 64);
    dw[16] = q2[0]; dw[17] = q2[1];
    wait_lgkm();
    const unsigned c2addr = (unsigned)(size_t)(W0 + 2 * CV_CSTR + 64 * lane);
#pragma unroll
    for (int i = 0; i < 8; ++i) {
      const int k = 2 * i;
      *(LAS u32x2*)(W0 + CV_CSTR + 64 * lane + 8 * i) = (u32x2){__builtin_amdgcn_alignbyte(dw[k + 1], dw[k], 2), __builtin_amdgcn_alignbyte(dw[k + 2], dw[k + 1], 2)};
      *(LAS u32x2*)(W0 + 3 * CV_CSTR + 64 * lane + 8 * i) = (u32x2){__builtin_amdgcn_alignbyte(dw[k + 2], dw[k + 1], 2), __builtin_amdgcn_alignbyte(dw[k + 3 < 18 ? k + 3 : 17], dw[k + 2], 2)};
      asm volatile("ds_write2_b32 %0, %1, %2 offset0:%3 offset1:%4" ::"v"(c2addr), "v"(dw[k + 1]), "v"(dw[k + 2]), "i"(k), "i"(k + 1) : "memory");
    }
    if (lane < 6) *(LAS u32x4*)(W0 + (1 + (lane >> 1)) * CV_CSTR + 4096 + 16 * (lane & 1)) = (u32x4){0u, 0u, 0u, 0u};
    if (lane < 32) *(LAS u32x4*)(W0 + CV_U + lane * 16) = (u32x4){0u, 0u, 0u, 0u};
#pragma unroll
    for (int q = 0; q < 4; ++q) *(LAS u32x4*)(W0 + CV_U + 512 + q * 1024 + lane * 16) = uv[q];
    wait_lgkm();
  }
  STAMP(4);
  const int p = lane & 15, g4 = lane >> 4;
  const int m00 = 2031 + 8 * g4 - p;
  const unsigned kaddr = base + (m00 & 3) * CV_CSTR + 2 * (m00 & ~3);
  const unsigned uaddr = base + CV_U + 512 + 2 * (16 * p - 16 + 8 * g4);
  f32x4 ac[8];
#pragma unroll
  for (int t = 0; t < 8; ++t) ac[t] = (f32x4){0.f, 0.f, 0.f, 0.f};
  bf16x8 uf[2][8], kf[3];
  auto rdu = [&](int r, int dl) LAM { uf[r & 1][dl] = lds_b128(smem, uaddr - 64 * r + 512 * dl); };
  auto rdk = [&](int tau) LAM {
    const int r = tau >> 3, i = (tau & 1) ? 7 - ((tau & 7) >> 1) : ((tau & 7) >> 1);
    const unsigned o = kaddr - 64 * r - 512 * i;
    const u32x2 k0 = lds_b64(smem, o), k1 = lds_b64(smem, o + 8);
    kf[tau % 3] = mk8(k0[0], k0[1], k1[0], k1[1]);
  };
#pragma unroll
  for (int dl = 0; dl < 8; ++dl) rdu(0, dl);
  rdk(0); rdk(1);
  sfor<0, 64>([&](auto S) LAM {
    constexpr int tau = S.v, r = tau >> 3, i = (tau & 1) ? 7 - ((tau & 7) >> 1) : ((tau & 7) >> 1);
    if constexpr (tau + 2 < 64) rdk(tau + 2);
    if constexpr (r < 7) rdu(r + 1, tau & 7);
#pragma unroll
    for (int t = i; t < 8; ++t) ac[t] = mfma16(kf[tau % 3], uf[r & 1][t - i], ac[t]);
    SCHED_BAR();
  });
  STAMP(5);
  const rsrc_t ra = mk_rsrc(ws + WS_AT, SL * DM * 2);
#pragma unroll
  for (int t = 0; t < 8; ++t) {
    const int l0 = 256 * t + 16 * p + 4 * g4;
    const u32x2 uu = lds_b64(smem, base + CV_U + 512 + 2 * l0);
    const unsigned y01 = pk2(ac[t][0], ac[t][1]), y23 = pk2(ac[t][2], ac[t][3]);
    const unsigned s01 = pk2(bflo(uu[0]) * dsk, bfhi(uu[0]) * dsk), s23 = pk2(bflo(uu[1]) * dsk, bfhi(uu[1]) * dsk);
    u32x2 o;
    o[0] = pk2(bflo(y01) + bflo(s01), bfhi(y01) + bfhi(s01));
    o[1] = pk2(bflo(y23) + bflo(s23), bfhi(y23) + bfhi(s23));
    st64(ra, (d * SL + l0) * 2, o);
  }
  drain();
}

DEV void s4_role(int j, const u16* __restrict__ x, const u16* Are, const u16* Aim, const u16* Bre, const u16* Bim,
                 const u16* Cre, const u16* Cim, const u16* logdt, const u16* Dskip, char* __restrict__ ws, char* smem) {
  const int tid = tidx(), lane = tid & 63;
  const int wv = __builtin_amdgcn_readfirstlane(tid >> 6);
  const int cg = j & 7, idx = j >> 3;
  const unsigned lds0 = (unsigned)(size_t)(LAS char*)smem;
  STAMP(0);
  if (wv == 3) {
    constexpr unsigned XT = 3 * CV_WAVE;
    const u16* xb = x + (size_t)64 * idx * DM + 80 * cg;
#pragma unroll
    for (int q = 0; q < 10; ++q) {
      const int ci = 64 * q + lane, rho = ci / 10, pos = ci - rho * 10;
      const int row = (rho & ~12) | ((rho & 4) << 1) | ((rho & 8) >> 1);
      glds_s(xb, (row * DM + pos * 8) * 2, lds0 + XT + q * 1024);
    }
    const u64 tag = read_tag(ws);
    wait_vm<0>();
    const int g4 = lane >> 4, c16 = lane & 15;
    const unsigned bo = XT + (16 * (g4 >> 1) + 4 * (g4 & 1) + (c16 >> 2)) * 160 + (lane & 3) * 8;
    const rsrc_t ru = mk_rsrc(ws + WS_UT, SL * DM * 2);
#pragma unroll
    for (int hf = 0; hf < 2; ++hf)
#pragma unroll
      for (int n = 0; n < 5; ++n) {
        const bf16x8 v = lds_tr2(smem, bo + hf * 5120 + n * 32, bo + hf * 5120 + 1280 + n * 32);
        st128(ru, ((80 * cg + 16 * n + c16) * SL + 64 * idx + 32 * hf + 8 * g4) * 2, __builtin_bit_cast(u32x4, v));
      }
    drain();
    if (lane == 0) flag_set(ws, WS_FU + (cg * 32 + idx) * 64, tag);
    STAMP(1);
    bar();
    return;
  }
  const u64 tag = read_tag(ws);
  const int nch = idx < 16 ? 3 : 2;
  const int d0 = 80 * cg + (idx < 16 ? 3 * idx : 48 + 2 * (idx - 16));
  if (wv < nch) s4_wave(d0 + wv, cg, Are, Aim, Bre, Bim, Cre, Cim, logdt, Dskip, ws, tag, smem, wv * CV_WAVE);
  bar();
  if (tid == 0) flag_set(ws, WS_FA + (cg * 32 + idx) * 64, tag);
  STAMP(6);
}

NOPK __global__ __launch_bounds__(256, 2) void fused_kernel(const u16* __restrict__ x, const u16* __restrict__ wqkv,
                                                            const u16* __restrict__ wo, const u16* __restrict__ gw,
                                                            const u16* Are, const u16* Aim, const u16* Bre, const u16* Bim,
                                                            const u16* Cre, const u16* Cim, const u16* logdt,
                                                            const u16* Dskip, char* __restrict__ ws, u16* __restrict__ out) {
  extern __shared__ __attribute__((aligned(16))) char smem[];
  const int b = bidx();
  if (b < 256) attn_role(b, x, wqkv, wo, gw, ws, out, smem);
  else s4_role(b - 256, x, Are, Aim, Bre, Bim, Cre, Cim, logdt, Dskip, ws, smem);
}

extern "C" void kernel_launch(void* const* d_in, const int* in_sizes, int n_in, void* d_out, int out_size, void* d_ws,
                              size_t ws_size, hipStream_t stream) {
  (void)hipFuncSetAttribute((const void*)fused_kernel, hipFuncAttributeMaxDynamicSharedMemorySize, LDS_BYTES);
  fused_kernel<<<512, 256, LDS_BYTES, stream>>>((const u16*)d_in[0], (const u16*)d_in[10], (const u16*)d_in[11],
                                                (const u16*)d_in[12], (const u16*)d_in[1], (const u16*)d_in[2],
                                                (const u16*)d_in[3], (const u16*)d_in[4], (const u16*)d_in[5],
                                                (const u16*)d_in[6], (const u16*)d_in[7], (const u16*)d_in[8],
                                                (char*)d_ws, (u16*)d_out);
}
```

```cpp
#include <hip/hip_runtime.h>
#include <hip/hip_bf16.h>
#include <stdint.h>

typedef unsigned short u16;
typedef unsigned long long u64;
typedef __attribute__((ext_vector_type(8))) __bf16 bf16x8;
typedef __attribute__((ext_vector_type(4))) __bf16 bf16x4;
typedef __attribute__((ext_vector_type(2))) __bf16 bf16x2;
typedef __attribute__((ext_vector_type(2))) float f32x2;
typedef __attribute__((ext_vector_type(4))) float f32x4;
typedef __attribute__((ext_vector_type(16))) float f32x16;
typedef __attribute__((ext_vector_type(2))) unsigned u32x2;
typedef __attribute__((ext_vector_type(4))) unsigned u32x4;
#define LAS __attribute__((address_space(3)))
#define GAS __attribute__((address_space(1)))
#if defined(__HIP_DEVICE_COMPILE__)
#define NOPK __attribute__((target("no-packed-fp32-ops")))
#else
#define NOPK
#endif
#define DEV __device__ __forceinline__ NOPK
#define LAM __attribute__((always_inline)) NOPK

constexpr int SL = 2048;
constexpr int DM = 640;
constexpr int NM = 192;

constexpr size_t WS_EPOCH = 0;
constexpr size_t WS_TMO = 2048;
constexpr size_t WS_FPART = 1u << 20;
constexpr size_t WS_FU = 4096;
constexpr size_t WS_FA = WS_FU + 16384;
constexpr size_t WS_FO = WS_FA + 16384;
constexpr size_t WS_PART = (1u << 20) + 65536;
constexpr size_t WS_O = 1u << 21;
constexpr size_t WS_UT = WS_O + (size_t)SL * DM * 2;
constexpr size_t WS_AT = WS_UT + (size_t)SL * DM * 2;
constexpr size_t WS_END = WS_AT + (size_t)SL * DM * 2;
constexpr int LDS_BYTES = 81920;
constexpr unsigned DD0 = 49152;

DEV float bf2f(unsigned h) { return __builtin_bit_cast(float, h << 16); }
DEV float bflo(unsigned w) { return __builtin_bit_cast(float, w << 16); }
DEV float bfhi(unsigned w) { return __builtin_bit_cast(float, w & 0xffff0000u); }
DEV int tidx() { return __builtin_amdgcn_workitem_id_x(); }
DEV int bidx() { return __builtin_amdgcn_workgroup_id_x(); }
DEV unsigned pk2(float a, float b) {
  f32x2 v = {a, b};
  return __builtin_bit_cast(unsigned, __builtin_convertvector(v, bf16x2));
}
DEV float rbf(float a) { return bflo(pk2(a, a)); }
DEV bf16x8 mk8(unsigned a, unsigned b, unsigned c, unsigned d) {
  u32x4 v = {a, b, c, d};
  return __builtin_bit_cast(bf16x8, v);
}
DEV f32x4 mfma16(bf16x8 a, bf16x8 b, f32x4 c) { return __builtin_amdgcn_mfma_f32_16x16x32_bf16(a, b, c, 0, 0, 0); }
DEV void mfma_ip(f32x4& c, bf16x8 a, bf16x8 b) { asm volatile("v_mfma_f32_16x16x32_bf16 %0, %1, %2, %0" : "+v"(c) : "v"(a), "v"(b)); }
DEV f32x16 mfma32(bf16x8 a, bf16x8 b, f32x16 c) { return __builtin_amdgcn_mfma_f32_32x32x16_bf16(a, b, c, 0, 0, 0); }
DEV bf16x8 lds_b128(const char* smem, unsigned off) { return *(const LAS bf16x8*)((const LAS char*)smem + off); }
DEV u32x2 lds_b64(const char* smem, unsigned off) { return *(const LAS u32x2*)((const LAS char*)smem + off); }
DEV bf16x8 lds_tr2(const char* smem, unsigned off0, unsigned off1) {
  bf16x4 t0 = __builtin_amdgcn_ds_read_tr16_b64_v4bf16((LAS bf16x4*)((LAS char*)smem + off0));
  bf16x4 t1 = __builtin_amdgcn_ds_read_tr16_b64_v4bf16((LAS bf16x4*)((LAS char*)smem + off1));
  return __builtin_shufflevector(t0, t1, 0, 1, 2, 3, 4, 5, 6, 7);
}
DEV void glds_s(const void* sbase, unsigned voff, unsigned ldsaddr) {
  asm volatile("s_mov_b32 m0, %2\n\ts_nop 0\n\tglobal_load_lds_dwordx4 %0, %1" ::"v"(voff), "s"(sbase), "s"(ldsaddr) : "memory");
}
DEV void glds_v(const void* vaddr, unsigned ldsaddr) {
  asm volatile("s_mov_b32 m0, %1\n\ts_nop 0\n\tglobal_load_lds_dwordx4 %0, off" ::"v"(vaddr), "s"(ldsaddr) : "memory");
}
#define FENCE() asm volatile("" ::: "memory")
#define SCHED_BAR() __builtin_amdgcn_sched_barrier(0)
#define SGB(mask, n) __builtin_amdgcn_sched_group_barrier(mask, n, 0)
DEV void bar() { FENCE(); __builtin_amdgcn_s_barrier(); FENCE(); }
template <int N> DEV void wait_vm() { asm volatile("s_waitcnt vmcnt(%0) lgkmcnt(0)" ::"i"(N) : "memory"); }
DEV void wait_lgkm() { asm volatile("s_waitcnt lgkmcnt(0)" ::: "memory"); }
template <int N> struct IC { static constexpr int v = N; };
template <int I, int E, class F> DEV void sfor(F&& f) { if constexpr (I < E) { f(IC<I>{}); sfor<I + 1, E>(f); } }

DEV float exp_acc(float x) {
  const float hi = x * 1.4426950408889634f;
  const float lo = __builtin_fmaf(x, 1.4426950408889634f, -hi) + x * 1.9259629911e-8f;
  const float e = __builtin_amdgcn_exp2f(hi);
  return __builtin_fmaf(e, lo * 0.6931471805599453f, e);
}
DEV float phi(float t) {
  float e = __builtin_amdgcn_exp2f(t * 1.4426950408889634f);
  e = __builtin_amdgcn_fmed3f(e, 0.f, 1.f);
  return __builtin_fmaf(0.5f, t + __builtin_fabsf(t), e + 1e-6f);
}

typedef __amdgpu_buffer_rsrc_t rsrc_t;
DEV rsrc_t mk_rsrc(const void* p, unsigned bytes) { return __builtin_amdgcn_make_buffer_rsrc((void*)p, 0, bytes, 0x00020000); }
constexpr int SC1 = 16;
DEV void st128(rsrc_t r, unsigned off, u32x4 v) { __builtin_amdgcn_raw_buffer_store_b128(v, r, off, 0, SC1); }
DEV void st64(rsrc_t r, unsigned off, u32x2 v) { __builtin_amdgcn_raw_buffer_store_b64(v, r, off, 0, SC1); }
DEV u32x4 ld128(rsrc_t r, unsigned off, unsigned soff = 0) { return __builtin_amdgcn_raw_buffer_load_b128(r, off, soff, SC1); }
DEV unsigned ld16(rsrc_t r, unsigned off, unsigned soff = 0) { return __builtin_amdgcn_raw_buffer_load_b16(r, off, soff, SC1); }
DEV u32x4 mk_srd(const void* p, unsigned bytes) {
  const u64 a = (u64)p;
  return (u32x4){(unsigned)__builtin_amdgcn_readfirstlane((int)(unsigned)a), (unsigned)__builtin_amdgcn_readfirstlane((int)(unsigned)(a >> 32)) & 0xffffu, bytes, 0x00020000u};
}
DEV void ld128_asm(u32x4& v, u32x4 srd, unsigned voff, unsigned soff) {
  asm volatile("buffer_load_dwordx4 %0, %1, %2, %3 offen sc1" : "=v"(v) : "v"(voff), "s"(srd), "s"(soff) : "memory");
}
DEV void drain() { asm volatile("s_waitcnt vmcnt(0)" ::: "memory"); }
DEV void flag_set(char* ws, size_t off, u64 tag) {
  __hip_atomic_store((GAS u64*)(ws + off), tag, __ATOMIC_RELAXED, __HIP_MEMORY_SCOPE_AGENT);
}
DEV void flags_wait(char* ws, size_t off, int n, u64 tag) {
  const int lane = tidx() & 63;
  GAS u64* p = (GAS u64*)(ws + off) + (lane < n ? lane : 0) * 8;
  for (unsigned spins = 0; spins < (1u << 16); ++spins) {
    const u64 v = __hip_atomic_load(p, __ATOMIC_RELAXED, __HIP_MEMORY_SCOPE_AGENT);
    if (__builtin_amdgcn_ballot_w64(v != tag) == 0) { FENCE(); return; }
    __builtin_amdgcn_s_sleep(1);
  }
  if (lane == 0) __hip_atomic_store((GAS unsigned*)(ws + WS_TMO), 1u, __ATOMIC_RELAXED, __HIP_MEMORY_SCOPE_AGENT);
  FENCE();
}
DEV void flags_wait_at(char* ws, unsigned off, u64 tag) {
  GAS u64* p = (GAS u64*)(ws + off);
  for (unsigned spins = 0; spins < (1u << 16); ++spins) {
    const u64 v = __hip_atomic_load(p, __ATOMIC_RELAXED, __HIP_MEMORY_SCOPE_AGENT);
    if (__builtin_amdgcn_ballot_w64(v != tag) == 0) { FENCE(); return; }
    __builtin_amdgcn_s_sleep(1);
  }
  if ((tidx() & 63) == 0) __hip_atomic_store((GAS unsigned*)(ws + WS_TMO), 1u, __ATOMIC_RELAXED, __HIP_MEMORY_SCOPE_AGENT);
  FENCE();
}
DEV u64 read_tag(const char* ws) {
  u64 e;
  asm volatile("s_load_dwordx2 %0, %1, 0x0\n\ts_waitcnt lgkmcnt(0)" : "=s"(e) : "s"(ws) : "memory");
  return e + 1;
}

constexpr int wait_count(int s) {
  const int needW = s - 3, needX = 2 * (s >> 1) + 1 - 4;
  const int need = needW > needX ? needW : needX;
  int c = 0;
  for (int t = need + 1; t < s; ++t) { if (t + 4 >= 0 && 2 * (t + 4) < 40) c += 2; if (t + 3 >= 0 && 2 * (t + 3) < 40) c += 2; }
  return c;
}

DEV void attn_role(int b, const u16* __restrict__ x, const u16* __restrict__ w, const u16* __restrict__ wo,
                   const u16* __restrict__ gw, char* __restrict__ ws, u16* __restrict__ out, char* smem) {
  const int tid = tidx(), lane = tid & 63;
  const int wv = __builtin_amdgcn_readfirstlane(tid >> 6);
  const int xcd = b & 7, ii = b >> 3;
  const int rt = 4 * (xcd >> 1) + (ii >> 3), g = 8 * (xcd & 1) + (ii & 7);
  const int gq = g ^ 1;
  const int c16 = lane & 15, g4 = lane >> 4;
  const unsigned lds0 = (unsigned)(size_t)(LAS char*)smem;

  constexpr unsigned WB = 3 * 16384;
  const u16* xb = x + (size_t)rt * 128 * DM;
  unsigned xo[4];
#pragma unroll
  for (int q = 0; q < 4; ++q) { const int row = 32 * wv + 8 * q + (lane >> 3); xo[q] = (row * DM + ((lane & 7) ^ ((row >> 1) & 7)) * 8) * 2; }
  const char* wp[2];
  int wstep[2];
#pragma unroll
  for (int q = 0; q < 2; ++q) {
    const int kr = 8 * wv + 4 * q + (lane >> 4);
    const int wc = (lane & 15) ^ (((kr & 3) << 2) | ((kr >> 2) & 3));
    const int seg = wc / 5;
    const int col = (seg == 0 ? DM + 40 * g : (seg == 1 ? 2 * DM + 40 * g : 40 * gq)) + (wc - seg * 5) * 8;
    const bool isg = wc == 15;
    wp[q] = isg ? (const char*)(gw + (kr & ~7)) : (const char*)(w + (size_t)kr * (3 * DM) + col);
    wstep[q] = isg ? 64 : 32 * 3 * DM * 2;
    wp[q] -= wstep[q];
  }
  auto xissue = [&](int p) LAM { if (p < 40) glds_s(xb + (p >> 2) * 64, xo[p & 3], lds0 + ((p >> 2) % 3) * 16384 + wv * 4096 + (p & 3) * 1024); };
  auto wissue = [&](int p) LAM {
    if (p < 40) {
      wp[p & 1] += wstep[p & 1];
      glds_v(wp[p & 1], lds0 + WB + ((p >> 1) % 4) * 8192 + (2 * wv + (p & 1)) * 1024);
    }
  };
  unsigned aoff[2];
  { const int row = 32 * wv + c16;
#pragma unroll
    for (int ks = 0; ks < 2; ++ks) aoff[ks] = row * 128 + 16 * ((4 * ks + g4) ^ ((row >> 1) & 7)); }
  unsigned boff[2][8];
  { const int q = c16 >> 2, p = lane & 3;
#pragma unroll
    for (int t = 0; t < 2; ++t) { const int kr = 8 * g4 + 4 * t + q; const int f = ((kr & 3) << 2) | ((kr >> 2) & 3);
#pragma unroll
      for (int n = 0; n < 8; ++n) boff[t][n] = (WB + kr * 256 + 16 * ((p >> 1) ^ f) + 8 * (p & 1)) ^ (n << 5); } }
  unsigned gmask[4];
#pragma unroll
  for (int r = 0; r < 4; ++r) gmask[r] = c16 < 8 ? 0xffffffffu : (c16 - 8 == 2 * r ? 0x0000ffffu : (c16 - 8 == 2 * r + 1 ? 0xffff0000u : 0u));

  sfor<0, 4>([&](auto S) LAM { constexpr int s = S.v - 4;
    xissue(2 * (s + 4)); xissue(2 * (s + 4) + 1);
    if constexpr (s + 3 >= 0) { wissue(2 * (s + 3)); wissue(2 * (s + 3) + 1); } });
  const u64 tag = read_tag(ws);
  unsigned psoff[2];
  unsigned woff5[5];
  unsigned kvoff;
  unsigned ooff[3];
  auto tail_addresses = [&]() LAM {
#pragma unroll
    for (int pp = 0; pp < 2; ++pp) {
      const int p = 2 * wv + pp;
      const int ta = p < 2 ? 0 : (p < 5 ? 1 : 2);
      const int tb = p < 2 ? 2 + p : (p < 5 ? p : p - 3);
      const int d = 16 * ta + 4 * g4;
      const int h = d >= 20;
      const int vc = 16 * tb + c16 - 40;
      int e;
      bool ok = d < 40;
      if (tb == 2 && c16 < 8) { e = 20; ok = ok && c16 == 0; }
      else { const int he = vc >= 20; e = vc - 20 * he; ok = ok && he == h; }
      psoff[pp] = ok ? (((g * 16 + rt) * 2 + h) * 420 + e * 20 + (d - 20 * h)) * 4 : 0x7fff0000u;
    }
#pragma unroll
    for (int i = 0; i < 5; ++i) {
      const int ci = 64 * i + lane, rho = ci / 10, pos = ci - rho * 10;
      const int kr = (rho & ~12) | ((rho & 4) << 1) | ((rho & 8) >> 1);
      woff5[i] = (kr * DM + pos * 8) * 2;
    }
    {
      const int h = tid / 105, rem = tid - h * 105, e = rem / 5, d0 = (rem - e * 5) * 4;
      kvoff = 76800 + (e < 20 ? 20 * h + e : 40 + h) * 96 + (20 * h + d0) * 2;
    }
#pragma unroll
    for (int T = 0; T < 3; ++T) {
      const int c8 = 5 * gq + 2 * T + (g4 >> 1);
      ooff[T] = (T < 2 || g4 < 2) ? (((8 * rt + 2 * wv + (g4 & 1)) * 20 + (c8 >> 2)) * 64 + (c8 & 3) * 16 + c16) * 16 : 0x7fff0000u;
    }
    asm volatile("" : "+v"(psoff[0]), "+v"(psoff[1]), "+v"(kvoff), "+v"(ooff[0]), "+v"(ooff[1]), "+v"(ooff[2]));
    asm volatile("" : "+v"(woff5[0]), "+v"(woff5[1]), "+v"(woff5[2]), "+v"(woff5[3]), "+v"(woff5[4]));
  };

  f32x4 acc[2][8];
#pragma unroll
  for (int m = 0; m < 2; ++m)
#pragma unroll
    for (int n = 0; n < 8; ++n) acc[m][n] = (f32x4){0.f, 0.f, 0.f, 0.f};
  bf16x8 fa[2][2], fb[2][8];
  auto rdA = [&](int par, int s) LAM {
#pragma unroll
    for (int m = 0; m < 2; ++m) fa[par][m] = lds_b128(smem, ((s >> 1) % 3) * 16384 + aoff[s & 1] + m * 2048);
  };
  auto rdB = [&](int par, int s, int n) LAM {
    fb[par][n] = lds_tr2(smem, (s % 4) * 8192 + boff[0][n], (s % 4) * 8192 + boff[1][n]);
  };
  auto mm = [&](int par, int n, int s) LAM {
    if (n == 7) {
      u32x4 v = __builtin_bit_cast(u32x4, fb[par][7]);
      v[0] &= gmask[0]; v[1] &= gmask[1]; v[2] &= gmask[2]; v[3] &= gmask[3];
      fb[par][7] = __builtin_bit_cast(bf16x8, v);
    }
#pragma unroll
    for (int m = 0; m < 2; ++m) acc[m][n] = n < 5 ? mfma16(fa[par][m], fb[par][n], acc[m][n]) : mfma16(fb[par][n], fa[par][m], acc[m][n]);
  };
#define GRP3() SGB(0x8, 1); SGB(0x100, 2); SGB(0x8, 1); SGB(0x100, 2); SGB(0x8, 1); SGB(0x100, 2); SGB(0x8, 1)
  sfor<0, 20>([&](auto S) LAM {
    constexpr int s = S.v;
    wait_vm<wait_count(s)>();
    bar();
    constexpr int cur = s & 1, prv = cur ^ 1;
    rdA(cur, s); rdB(cur, s, 0); rdB(cur, s, 1);
    if constexpr (s > 0) { mm(prv, 0, s - 1); mm(prv, 1, s - 1); GRP3(); }
    SCHED_BAR();
    wissue(2 * (s + 3));
    SCHED_BAR();
    rdB(cur, s, 2); rdB(cur, s, 3); rdB(cur, s, 4);
    if constexpr (s > 0) { mm(prv, 2, s - 1); mm(prv, 3, s - 1); GRP3(); }
    SCHED_BAR();
    wissue(2 * (s + 3) + 1);
    SCHED_BAR();
    rdB(cur, s, 5); rdB(cur, s, 6); rdB(cur, s, 7);
    if constexpr (s > 0) { mm(prv, 4, s - 1); mm(prv, 5, s - 1); GRP3(); }
    SCHED_BAR();
    xissue(2 * (s + 4));
    SCHED_BAR();
    if constexpr (s > 0) mm(prv, 6, s - 1);
    SCHED_BAR();
    xissue(2 * (s + 4) + 1);
    SCHED_BAR();
    if constexpr (s > 0) mm(prv, 7, s - 1);
    SCHED_BAR();
    if constexpr (s == 0) { tail_addresses(); SCHED_BAR(); }
  });
  wait_lgkm();
  bar();
  const u16* wob = wo + 80 * (g & 7);
  auto wo_piece = [&](int step, unsigned dst, int i) LAM { glds_s(wob + (size_t)step * 32 * DM, woff5[i], lds0 + dst + i * 1024); };
#pragma unroll
  for (int n = 0; n < 8; ++n) mm(1, n, 19);

  {
    bf16x8 fr[5], v2;
#pragma unroll
    for (int n = 0; n < 5; ++n) {
      unsigned u[4];
#pragma unroll
      for (int m = 0; m < 2; ++m)
#pragma unroll
        for (int j = 0; j < 2; ++j) {
          float a0 = acc[m][n][2 * j], a1 = acc[m][n][2 * j + 1];
          if (n < 2) { a0 = phi(a0); a1 = phi(a1); }
          if (n == 2) { a0 = c16 < 8 ? phi(a0) : a0; a1 = c16 < 8 ? phi(a1) : a1; }
          u[2 * m + j] = pk2(a0, a1);
        }
      fr[n] = mk8(u[0], u[1], u[2], u[3]);
      if (n == 2) { const unsigned one = 0x3f803f80u; v2 = c16 < 8 ? mk8(one, one, one, one) : fr[2]; }
    }
    const f32x4 z4 = {0.f, 0.f, 0.f, 0.f};
    f32x4 dd[8];
    dd[0] = mfma16(fr[0], v2, z4);
    dd[1] = mfma16(fr[0], fr[3], z4);
    dd[2] = mfma16(fr[1], v2, z4);
    dd[3] = mfma16(fr[1], fr[3], z4);
    dd[4] = mfma16(fr[1], fr[4], z4);
    dd[5] = mfma16(fr[2], v2, z4);
    dd[6] = mfma16(fr[2], fr[3], z4);
    dd[7] = mfma16(fr[2], fr[4], z4);
#pragma unroll
    for (int p = 0; p < 8; ++p) *(LAS f32x4*)((LAS char*)smem + DD0 + wv * 8192 + p * 1024 + lane * 16) = dd[p];
  }
  wait_lgkm();
  bar();
  {
    f32x4 ps[2];
#pragma unroll
    for (int pp = 0; pp < 2; ++pp) {
      const unsigned o = DD0 + (2 * wv + pp) * 1024 + lane * 16;
      ps[pp] = *(const LAS f32x4*)((LAS char*)smem + o);
#pragma unroll
      for (int w2 = 1; w2 < 4; ++w2) ps[pp] += *(const LAS f32x4*)((LAS char*)smem + w2 * 8192 + o);
    }
    const rsrc_t rp = mk_rsrc(ws + WS_PART, 16 * 16 * 3360);
#pragma unroll
    for (int pp = 0; pp < 2; ++pp) st128(rp, psoff[pp], __builtin_bit_cast(u32x4, ps[pp]));
  }
  FENCE();
  drain();
  if (lane == 0) flag_set(ws, WS_FPART + ((g * 16 + rt) * 4 + wv) * 64, tag);

  bar();
#pragma unroll
  for (int q = 0; q < 5; ++q) wo_piece(16 + wv, 5120 * (11 + wv), q);
  if (wv < 3) {
#pragma unroll
    for (int i = 0; i < 3; ++i)
#pragma unroll
      for (int q = 0; q < 5; ++q) wo_piece(wv + 3 * i, 5120 * (wv + 3 * i), q);
  }
  SCHED_BAR();
  bf16x8 p01[2], p2[2];
#pragma unroll
  for (int m = 0; m < 2; ++m) {
    p01[m] = mk8(pk2(phi(acc[m][5][0]), phi(acc[m][5][1])), pk2(phi(acc[m][5][2]), phi(acc[m][5][3])),
                 pk2(phi(acc[m][6][0]), phi(acc[m][6][1])), pk2(phi(acc[m][6][2]), phi(acc[m][6][3])));
    const unsigned a = pk2(phi(acc[m][7][0]), phi(acc[m][7][1])), c = pk2(phi(acc[m][7][2]), phi(acc[m][7][3]));
    p2[m] = mk8(g4 < 2 ? a : 0u, g4 < 2 ? c : 0u, 0u, 0u);
  }
  {
    LAS float* gl = (LAS float*)((LAS char*)smem + 80896);
#pragma unroll
    for (int m = 0; m < 2; ++m) {
      const float part0 = (acc[m][7][0] + acc[m][7][1]) + (acc[m][7][2] + acc[m][7][3]);
      const float sum = part0 + __builtin_bit_cast(float, __builtin_amdgcn_ds_bpermute((lane ^ 16) << 2, __builtin_bit_cast(int, part0)));
      const float gg = rbf(__builtin_amdgcn_rcpf(1.f + __builtin_amdgcn_exp2f(-1.4426950408889634f * rbf(sum))));
      if (g4 == 2) gl[32 * wv + 16 * m + c16] = gg;
    }
    *(LAS u32x4*)((LAS char*)smem + 76800 + tid * 16) = (u32x4){0u, 0u, 0u, 0u};
  }
  asm volatile("" : "+v"(p01[0]), "+v"(p01[1]), "+v"(p2[0]), "+v"(p2[1]));
  if (wv < 3) asm volatile("s_waitcnt vmcnt(15) lgkmcnt(0)" ::: "memory");
  else { flags_wait(ws, WS_FPART + gq * 64 * 64, 64, tag); wait_vm<0>(); }
  bar();

  const int half = g >> 3;
  unsigned boff3 = (16 * (g4 >> 1) + 4 * (g4 & 1) + (c16 >> 2)) * 160 + (lane & 3) * 8;
  unsigned boff3h = boff3 + 51200;
  asm volatile("" : "+v"(boff3), "+v"(boff3h));
  bf16x8 wr[4][5];
  float gate;
  {
    const u32x4 srd = mk_srd(ws + WS_PART + (size_t)gq * 16 * 3360, 16 * 3360);
    const unsigned voff = tid < 210 ? tid * 16 : 0x7fff0000u;
    u32x4 v[16];
#pragma unroll
    for (int r = 0; r < 16; ++r) ld128_asm(v[r], srd, voff, r * 3360);
    if (wv < 2) {
#pragma unroll
      for (int q = 0; q < 5; ++q) wo_piece(9 + wv, 5120 * (9 + wv), q);
    }
#pragma unroll
    for (int i = 0; i < 4; ++i)
#pragma unroll
      for (int n = 0; n < 5; ++n) wr[i][n] = lds_tr2(smem, 5120 * (1 + i) + boff3h + n * 32, 5120 * (1 + i) + boff3h + 1280 + n * 32);
    gate = *(const LAS float*)((LAS char*)smem + 80896 + (64 * half + 16 * wv + c16) * 4);
    if (wv < 2) asm volatile("s_waitcnt vmcnt(5)" ::: "memory"); else asm volatile("s_waitcnt vmcnt(0)" ::: "memory");
    asm volatile("" : "+v"(v[0]), "+v"(v[1]), "+v"(v[2]), "+v"(v[3]), "+v"(v[4]), "+v"(v[5]), "+v"(v[6]), "+v"(v[7]));
    asm volatile("" : "+v"(v[8]), "+v"(v[9]), "+v"(v[10]), "+v"(v[11]), "+v"(v[12]), "+v"(v[13]), "+v"(v[14]), "+v"(v[15]));
    f32x4 s = __builtin_bit_cast(f32x4, v[0]);
#pragma unroll
    for (int r = 1; r < 16; ++r) s += __builtin_bit_cast(f32x4, v[r]);
    if (tid < 210) *(LAS u32x2*)((LAS char*)smem + kvoff) = (u32x2){pk2(s[0], s[1]), pk2(s[2], s[3])};
    wait_lgkm();
    bar();
    const f32x4 z4 = {0.f, 0.f, 0.f, 0.f};
    auto afr = [&](int row, bf16x8& a01, bf16x8& a2) LAM {
      const unsigned o = 76800 + row * 96 + 8 * g4;
      const u32x2 t0 = lds_b64(smem, o), t1 = lds_b64(smem, o + 32), t2 = lds_b64(smem, o + 64);
      a01 = mk8(t0[0], t0[1], t1[0], t1[1]);
      a2 = mk8(g4 < 2 ? t2[0] : 0u, g4 < 2 ? t2[1] : 0u, 0u, 0u);
    };
    bf16x8 k01[3], k2[3], z01[2], z2[2];
#pragma unroll
    for (int T = 0; T < 3; ++T) afr(T < 2 ? 16 * T + c16 : 32 + (c16 & 7), k01[T], k2[T]);
    afr(40, z01[0], z2[0]);
    afr(41, z01[1], z2[1]);
    u32x2 ov[2][3];
#pragma unroll
    for (int m = 0; m < 2; ++m) {
      const f32x4 d0 = mfma16(z01[0], p01[m], z4);
      const f32x4 d1 = mfma16(z2[1], p2[m], mfma16(z01[1], p01[m], z4));
      const float inv0 = __builtin_amdgcn_rcpf(d0[0] + 1e-6f), inv1 = __builtin_amdgcn_rcpf(d1[0] + 1e-6f);
#pragma unroll
      for (int T = 0; T < 3; ++T) {
        const f32x4 nn = mfma16(k2[T], p2[m], mfma16(k01[T], p01[m], z4));
        const float inv = T == 0 ? inv0 : (T == 2 ? inv1 : (g4 == 0 ? inv0 : inv1));
        ov[m][T][0] = pk2(nn[0] * inv, nn[1] * inv);
        ov[m][T][1] = pk2(nn[2] * inv, nn[3] * inv);
      }
    }
    const rsrc_t ro = mk_rsrc(ws + WS_O, SL * DM * 2);
#pragma unroll
    for (int T = 0; T < 3; ++T) {
      const auto s0 = __builtin_amdgcn_permlane16_swap(ov[0][T][0], ov[1][T][0], false, false);
      const auto s1 = __builtin_amdgcn_permlane16_swap(ov[0][T][1], ov[1][T][1], false, false);
      const u32x4 v = {s0[0], s1[0], s0[1], s1[1]};
      st128(ro, ooff[T], v);
    }
  }
  bar();
  drain();
  if (lane == 0) flag_set(ws, WS_FO + ((rt * 4 + wv) * 16 + gq) * 64, tag);
  if (wv < 3) {
#pragma unroll
    for (int q = 0; q < 5; ++q) wo_piece(11 + wv, 5120 * (11 + wv), q);
    if (wv == 0) {
#pragma unroll
      for (int q = 0; q < 4; ++q) wo_piece(14, 5120 * 14, q);
    } else if (wv == 1) {
      wo_piece(14, 5120 * 14, 4); wo_piece(15, 5120 * 15, 0); wo_piece(15, 5120 * 15, 1);
    } else {
      wo_piece(15, 5120 * 15, 2); wo_piece(15, 5120 * 15, 3); wo_piece(15, 5120 * 15, 4);
    }
  }

  const int ct = g & 7;
  const int row0 = 128 * rt + 64 * half + 16 * wv;
  if (wv == 3)
    flags_wait_at(ws, lane >= 32 ? (unsigned)WS_FA + (ct * 32 + lane - 32) * 64
                                 : (unsigned)WS_FO + ((rt * 4 + 2 * half + (lane >> 4)) * 16 + (lane & 15)) * 64, tag);
  wait_vm<0>();
  bar();

  u32x4 of[20];
  {
    const rsrc_t ro = mk_rsrc(ws + WS_O, SL * DM * 2);
#pragma unroll
    for (int y = 0; y < 4; ++y) {
      of[16 + y] = ld128(ro, ((row0 >> 4) * 20 * 64 + lane) * 16, (16 + y) * 1024);
#pragma unroll
      for (int i = 0; i < 4; ++i) of[4 * y + i] = ld128(ro, ((row0 >> 4) * 20 * 64 + lane) * 16, (4 * y + i) * 1024);
    }
  }
  u64 fa_chk[4];
  if (b == 0 && wv == 0) {
#pragma unroll
    for (int i = 0; i < 4; ++i) fa_chk[i] = __hip_atomic_load((GAS u64*)(ws + WS_FA) + (64 * i + lane) * 8, __ATOMIC_RELAXED, __HIP_MEMORY_SCOPE_AGENT);
  }
  unsigned av[5][4];
  const rsrc_t ra = mk_rsrc(ws + WS_AT, SL * DM * 2);
  const unsigned aoff0 = ((80 * ct + 4 * g4) * SL + row0 + c16) * 2;
  SCHED_BAR();
  f32x4 oc[5];
#pragma unroll
  for (int n = 0; n < 5; ++n) oc[n] = (f32x4){0.f, 0.f, 0.f, 0.f};
  asm volatile("s_nop 1" : "+v"(oc[0]), "+v"(oc[1]), "+v"(oc[2]), "+v"(oc[3]), "+v"(oc[4]));
  {
    bf16x8 wf[6];
    auto rdw = [&](int q) LAM {
      const int s = q / 5, n = q - 5 * s;
      const unsigned o = (s < 10 ? 5120 * s + boff3 : 5120 * (s - 10) + boff3h) + n * 32;
      wf[q % 6] = lds_tr2(smem, o, o + 1280);
    };
#pragma unroll
    for (int q = 0; q < 5; ++q) rdw(q);
    sfor<0, 80>([&](auto Q) LAM {
      constexpr int q = Q.v, s = q / 5, n = q % 5;
      if constexpr (q % 4 == 0) {
        constexpr int y = q / 20, n2 = (q % 20) / 4;
        mfma_ip(oc[n2], wr[y][n2], __builtin_bit_cast(bf16x8, of[16 + y]));
      }
      if constexpr (q + 5 < 80) rdw(q + 5);
      if constexpr (q >= 2 && q < 42 && (q & 1) == 0) { constexpr int i = (q - 2) / 2; av[i / 4][i % 4] = ld16(ra, aoff0, (16 * (i / 4) + (i % 4)) * SL * 2); }
      mfma_ip(oc[n], wf[q % 6], __builtin_bit_cast(bf16x8, of[s]));
      SCHED_BAR();
    });
    asm volatile("s_nop 7" : "+v"(oc[0]), "+v"(oc[1]), "+v"(oc[2]), "+v"(oc[3]), "+v"(oc[4]));
  }
  {
    const float g1 = 1.f - gate;
    u32x2 r[5];
#pragma unroll
    for (int n = 0; n < 5; ++n) {
      float t[4];
#pragma unroll
      for (int j = 0; j < 4; ++j) t[j] = __builtin_fmaf(gate, bf2f(av[n][j]), g1 * oc[n][j]);
      r[n][0] = pk2(t[0], t[1]); r[n][1] = pk2(t[2], t[3]);
    }
    u16* dst = out + (size_t)(row0 + c16) * DM + 80 * ct;
#pragma unroll
    for (int n = 0; n < 4; n += 2) {
      const auto s0 = __builtin_amdgcn_permlane16_swap(r[n][0], r[n + 1][0], false, false);
      const auto s1 = __builtin_amdgcn_permlane16_swap(r[n][1], r[n + 1][1], false, false);
      const u32x4 v = {s0[0], s1[0], s0[1], s1[1]};
      *(u32x4*)(dst + 16 * (n + (g4 & 1)) + 8 * (g4 >> 1)) = v;
    }
    *(u32x2*)(dst + 64 + 4 * g4) = r[4];
  }
  if (b == 0 && wv == 0) {
    bool all = true;
#pragma unroll
    for (int i = 0; i < 4; ++i) all = all && fa_chk[i] == tag;
    if (__builtin_amdgcn_ballot_w64(!all) != 0) {
#pragma unroll 1
      for (int i = 0; i < 4; ++i) flags_wait(ws, WS_FA + i * 64 * 64, 64, tag);
    }
    if (lane == 0) __hip_atomic_store((GAS u64*)(ws + WS_EPOCH), tag, __ATOMIC_RELAXED, __HIP_MEMORY_SCOPE_AGENT);
  }
}

struct cf { float r, i; };
DEV cf cmul(cf a, cf b) { return {a.r * b.r - a.i * b.i, a.r * b.i + a.i * b.r}; }
DEV cf cexp_s(float a, float b, float s) {
  const float mag = __builtin_amdgcn_exp2f(a * (s * 1.4426950408889634f));
  const float rev = __builtin_amdgcn_fractf(b * (s * 0.15915494309189535f));
  return {mag * __builtin_amdgcn_cosf(rev), mag * __builtin_amdgcn_sinf(rev)};
}

constexpr unsigned CV_CSTR = 4160;
constexpr unsigned CV_U = 4 * CV_CSTR;
constexpr unsigned CV_WAVE = CV_U + 512 + 4096 + 256;

DEV void l2_prefetch(const u16* x, const u16* wq, int cg, int id0, int stride, unsigned (&pf)[2]) {
#pragma unroll
  for (int i = 0; i < 2; ++i) {
    const int id1 = id0 + i * stride + (tidx() & 63), id = id1 < 14720 ? id1 : 14719;
    const int s = id / 1472, r = id - s * 1472;
    const int r2 = r - 512, kr = s * 64 + r2 / 15, l = r2 % 15, seg = l / 5;
    const char* px = (const char*)x + ((4 * (cg >> 1) * 128 + r) * DM + s * 64) * 2;
    const char* pw = (const char*)wq + (kr * 3 * DM + seg * DM + 320 * (cg & 1)) * 2 + (l - seg * 5) * 128;
    const char* p = r < 512 ? px : pw;
    pf[i] = *(const GAS unsigned*)p;
  }
}

DEV void s4_wave(int d, int cg, const u16* __restrict__ x, const u16* __restrict__ wq, const u16* __restrict__ Are, const u16* __restrict__ Aim, const u16* __restrict__ Bre,
                 const u16* __restrict__ Bim, const u16* __restrict__ Cre, const u16* __restrict__ Cim,
                 const u16* __restrict__ logdt, const u16* __restrict__ Dskip, char* __restrict__ ws, u64 tag,
                 char* smem, unsigned base) {
  const int lane = tidx() & 63;
  unsigned long long kd0;
  asm volatile("s_memrealtime %0\n\ts_waitcnt lgkmcnt(0)" : "=s"(kd0)::"memory");
  u16 pa[3][6];
#pragma unroll
  for (int c = 0; c < 3; ++c) {
    const int idx = d * NM + c * 64 + lane;
    pa[c][0] = Are[idx]; pa[c][1] = Aim[idx]; pa[c][2] = Bre[idx];
    pa[c][3] = Bim[idx]; pa[c][4] = Cre[idx]; pa[c][5] = Cim[idx];
  }
  const unsigned ldt = logdt[d], dskw = Dskip[d];
  const float dt = rbf(exp_acc(bf2f(ldt)));
  const float dsk = bf2f(dskw);
  asm volatile("" ::"v"(dt), "v"(pa[0][5]));
  unsigned pf[2];
  l2_prefetch(x, wq, cg, 6144 + (d - 80 * cg) * 64, 5120, pf);
  FENCE();
  constexpr unsigned HI = 0, LO = 8192;
  const int r32 = lane & 31, hh = lane >> 5;
  const unsigned waddr = base + 128 * lane + 16 * (lane & 7);
  unsigned raddr[2];
#pragma unroll
  for (int t = 0; t < 2; ++t) {
    const int q = (lane & 15) >> 2, p = lane & 3, blk = (lane >> 4) & 1;
    const int n = 4 * hh + 2 * t + (q >> 1);
    raddr[t] = base + 128 * n + 16 * ((4 * (q & 1) + 2 * blk + (p >> 1)) ^ n) + 8 * (p & 1);
  }
  f32x16 acc;
#pragma unroll
  for (int i = 0; i < 16; ++i) acc[i] = 0.f;
  for (;;) {
    unsigned long long t;
    asm volatile("s_memrealtime %0\n\ts_waitcnt lgkmcnt(0)" : "=s"(t)::"memory");
    if (t - kd0 >= 300) break;
    __builtin_amdgcn_s_sleep(8);
  }
  bf16x8 fa[8], fb[8];
  u64 fu_seen = 0;
  sfor<0, 3>([&](auto C) LAM {
    constexpr int c = C.v;
    if constexpr (c == 2)
      fu_seen = __hip_atomic_load((GAS u64*)(ws + WS_FU + cg * 32 * 64) + (lane & 31) * 8, __ATOMIC_RELAXED, __HIP_MEMORY_SCOPE_AGENT);
    const float a = dt * bf2f(pa[c][0]), b = dt * bf2f(pa[c][1]);
    const cf dB = {dt * bf2f(pa[c][2]), dt * bf2f(pa[c][3])};
    const cf cc = cmul({bf2f(pa[c][4]), bf2f(pa[c][5])}, dB);
    const cf w1 = cexp_s(a, b, 1.f), w256 = cexp_s(a, b, 256.f);
    const cf v8 = cexp_s(a, -b, 8.f), v64 = cexp_s(a, -b, 64.f), v128 = cexp_s(a, -b, 128.f);
    const cf w2 = cmul(w1, w1), w4 = cmul(w2, w2), v192 = cmul(v128, v64);
#define PUT8(tab, rg, v0, v1, v2, v3, v4, v5, v6, v7) do { \
      *(LAS u32x4*)((LAS char*)smem + (waddr ^ ((rg) << 4)) + (tab)) = (u32x4){pk2(v0.r, v1.r), pk2(v2.r, v3.r), pk2(v4.r, v5.r), pk2(v6.r, v7.r)}; \
      *(LAS u32x4*)((LAS char*)smem + (waddr ^ ((4 + (rg)) << 4)) + (tab)) = (u32x4){pk2(v0.i, v1.i), pk2(v2.i, v3.i), pk2(v4.i, v5.i), pk2(v6.i, v7.i)}; } while (0)
    const cf ya0 = {1.f, 0.f}, yb0 = v64, yc0 = v128, yd0 = v192, t0 = cc, A0 = cc;
    SCHED_BAR();
    if constexpr (c > 0) acc = mfma32(fa[0], fb[0], acc);
    const cf ya1 = v8, yb1 = cmul(yb0, v8), yc1 = cmul(yc0, v8), yd1 = cmul(yd0, v8), t1 = cmul(t0, w1), A1 = cmul(A0, w256);
    SCHED_BAR();
    if constexpr (c > 0) acc = mfma32(fa[1], fb[1], acc);
    const cf ya2 = cmul(ya1, v8), yb2 = cmul(yb1, v8), yc2 = cmul(yc1, v8), yd2 = cmul(yd1, v8), t2 = cmul(t1, w1), A2 = cmul(A1, w256);
    SCHED_BAR();
    if constexpr (c > 0) acc = mfma32(fa[2], fb[2], acc);
    const cf ya3 = cmul(ya2, v8), yb3 = cmul(yb2, v8), yc3 = cmul(yc2, v8), yd3 = cmul(yd2, v8), t3 = cmul(t2, w1), A3 = cmul(A2, w256);
    SCHED_BAR();
    if constexpr (c > 0) acc = mfma32(fa[3], fb[3], acc);
    const cf ya4 = cmul(ya3, v8), yb4 = cmul(yb3, v8), yc4 = cmul(yc3, v8), yd4 = cmul(yd3, v8), t4 = cmul(t3, w1), A4 = cmul(A3, w256);
    SCHED_BAR();
    if constexpr (c > 0) acc = mfma32(fa[4], fb[4], acc);
    const cf ya5 = cmul(ya4, v8), yb5 = cmul(yb4, v8), yc5 = cmul(yc4, v8), yd5 = cmul(yd4, v8), t5 = cmul(t4, w1), A5 = cmul(A4, w256);
    SCHED_BAR();
    if constexpr (c > 0) acc = mfma32(fa[5], fb[5], acc);
    const cf ya6 = cmul(ya5, v8), yb6 = cmul(yb5, v8), yc6 = cmul(yc5, v8), yd6 = cmul(yd5, v8), t6 = cmul(t5, w1), A6 = cmul(A5, w256);
    SCHED_BAR();
    if constexpr (c > 0) acc = mfma32(fa[6], fb[6], acc);
    const cf ya7 = cmul(ya6, v8), yb7 = cmul(yb6, v8), yc7 = cmul(yc6, v8), yd7 = cmul(yd6, v8), t7 = cmul(t6, w1), A7 = cmul(A6, w256);
    SCHED_BAR();
    if constexpr (c > 0) acc = mfma32(fa[7], fb[7], acc);
    const cf A8 = cmul(A7, w256);
    wait_lgkm();
    PUT8(LO, 0, ya0, ya1, ya2, ya3, ya4, ya5, ya6, ya7);
    PUT8(LO, 1, yb0, yb1, yb2, yb3, yb4, yb5, yb6, yb7);
    PUT8(LO, 2, yc0, yc1, yc2, yc3, yc4, yc5, yc6, yc7);
    PUT8(LO, 3, yd0, yd1, yd2, yd3, yd4, yd5, yd6, yd7);
    PUT8(HI, 0, t0, t1, t2, t3, t4, t5, t6, t7);
    {
      const cf h1 = cmul(A1, w2), h5 = cmul(A2, w4), h7 = cmul(A3, w4), h2 = cmul(h1, w2), h3 = cmul(h2, w2);
      PUT8(HI, 1, A1, h1, h2, h3, A2, h5, A3, h7);
      PUT8(HI, 2, A4, A5, A6, A7, A8, A8, A8, A8);
    }
    wait_lgkm();
#pragma unroll
    for (int ks = 0; ks < 8; ++ks) {
      fa[ks] = lds_tr2(smem, raddr[0] + HI + 1024 * ks, raddr[1] + HI + 1024 * ks);
      fb[ks] = lds_tr2(smem, raddr[0] + LO + 1024 * ks, raddr[1] + LO + 1024 * ks);
    }
    SCHED_BAR();
#undef PUT8
  });
  if (__builtin_amdgcn_ballot_w64(fu_seen != tag) != 0) flags_wait(ws, WS_FU + cg * 32 * 64, 32, tag);
  u32x4 uv[4];
  {
    const rsrc_t ru = mk_rsrc(ws + WS_UT, SL * DM * 2);
#pragma unroll
    for (int q = 0; q < 4; ++q) uv[q] = ld128(ru, (d * SL + lane * 8) * 2, q * 1024);
  }
  SCHED_BAR();
#pragma unroll
  for (int ks = 0; ks < 8; ++ks) acc = mfma32(fa[ks], fb[ks], acc);
  wait_lgkm();
  asm volatile("" ::"v"(pf[0]), "v"(pf[1]));
  __builtin_amdgcn_s_setprio(1);
  LAS char* W0 = (LAS char*)smem + base;
  {
    const int j = r32;
    LAS char* T = W0 + 3 * CV_CSTR;
    u32x2 v;
    v[0] = pk2(acc[0], acc[1]); v[1] = pk2(acc[2], acc[3]);
    *(LAS u32x2*)(T + 2 * (8 * j + 4 * hh)) = v;
    v[0] = pk2(acc[4], acc[5]); v[1] = pk2(acc[6], acc[7]);
    if (hh == 0) *(LAS u32x2*)(T + 2 * (256 + 4 * j)) = v;
    else { *(LAS unsigned*)(T + 2 * (384 + 2 * j)) = v[0]; *(LAS unsigned*)(T + 2 * (448 + 2 * j)) = v[1]; }
    v[0] = pk2(acc[8], acc[9]); v[1] = pk2(acc[10], acc[11]);
    if (hh == 0) {
      *(LAS u16*)(T + 2 * (512 + j)) = (u16)v[0];
      *(LAS u16*)(T + 2 * (544 + j)) = (u16)(v[0] >> 16);
      *(LAS u16*)(T + 2 * (576 + j)) = (u16)v[1];
      *(LAS u16*)(T + 2 * (608 + j)) = (u16)(v[1] >> 16);
    } else if (j == 0) {
      *(LAS u16*)(T + 2 * 640) = (u16)v[0];
    }
    wait_lgkm();
    auto rd3 = [&](int idx, unsigned& a, unsigned& b, unsigned& c) LAM {
      const unsigned ab = *(const LAS unsigned*)(T + 2 * idx);
      a = ab & 0xffffu; b = ab >> 16;
      c = *(const LAS u16*)(T + 2 * (idx + 2));
    };
    unsigned a, b, c;
    rd3(512 + 2 * lane, a, b, c);
    {
      const unsigned aa = a | (a << 16), bb = b | (b << 16), c2 = c | (c << 16), ba = b | (a << 16);
      *(LAS u32x4*)(W0 + 2 * (1008 - 16 * lane)) = (u32x4){c2, c2, bb, bb};
      *(LAS u32x4*)(W0 + 2 * (1016 - 16 * lane)) = (u32x4){bb, ba, aa, aa};
    }
    rd3(384 + 2 * lane, a, b, c);
    *(LAS u32x4*)(W0 + 2 * (1528 - 8 * lane)) = (u32x4){c | (c << 16), b | (b << 16), b | (a << 16), a | (a << 16)};
    rd3(256 + 2 * lane, a, b, c);
    *(LAS u32x2*)(W0 + 2 * (1788 - 4 * lane)) = (u32x2){c | (b << 16), a | (a << 16)};
    const u32x2 t = *(const LAS u32x2*)(T + 2 * (4 * lane));
    *(LAS u32x2*)(W0 + 2 * (2044 - 4 * lane)) = (u32x2){(t[1] >> 16) | (t[1] << 16), (t[0] >> 16) | (t[0] << 16)};
    if (lane < 2) *(LAS u32x4*)(W0 + 4096 + 16 * lane) = (u32x4){0u, 0u, 0u, 0u};
    wait_lgkm();
    u32x4 d4[4];
    u32x2 d2[4];
#pragma unroll
    for (int i = 0; i < 4; ++i) {
      d4[i] = *(const LAS u32x4*)(W0 + 1024 * i + 16 * lane);
      d2[i] = *(const LAS u32x2*)(W0 + 1024 * i + 16 * lane + 16);
    }
#pragma unroll
    for (int i = 0; i < 4; ++i) {
      const unsigned dw[6] = {d4[i][0], d4[i][1], d4[i][2], d4[i][3], d2[i][0], d2[i][1]};
      u32x4 c1, c2, c3;
#pragma unroll
      for (int e = 0; e < 4; ++e) {
        c1[e] = __builtin_amdgcn_alignbyte(dw[e + 1], dw[e], 2);
        c3[e] = __builtin_amdgcn_alignbyte(dw[e + 2], dw[e + 1], 2);
        asm("v_mov_b32 %0, %1" : "=v"(c2[e]) : "v"(dw[e + 1]));
      }
      *(LAS u32x4*)(W0 + CV_CSTR + 1024 * i + 16 * lane) = c1;
      *(LAS u32x4*)(W0 + 2 * CV_CSTR + 1024 * i + 16 * lane) = c2;
      *(LAS u32x4*)(W0 + 3 * CV_CSTR + 1024 * i + 16 * lane) = c3;
    }
    if (lane < 6) *(LAS u32x4*)(W0 + (1 + (lane >> 1)) * CV_CSTR + 4096 + 16 * (lane & 1)) = (u32x4){0u, 0u, 0u, 0u};
    if (lane < 32) *(LAS u32x4*)(W0 + CV_U + lane * 16) = (u32x4){0u, 0u, 0u, 0u};
#pragma unroll
    for (int q = 0; q < 4; ++q) *(LAS u32x4*)(W0 + CV_U + 512 + q * 1024 + lane * 16) = uv[q];
    wait_lgkm();
  }
  const int p = lane & 15, g4 = lane >> 4;
  const int m00 = 2031 + 8 * g4 - p;
  unsigned kaddr = base + (m00 & 3) * CV_CSTR + 2 * (m00 & ~3) - 64 * 7 - 512 * 7;
  unsigned uaddr = base + CV_U + 512 + 2 * (16 * p - 16 + 8 * g4) - 64 * 7;
  asm volatile("" : "+v"(kaddr), "+v"(uaddr));
  f32x4 ac[8];
#pragma unroll
  for (int t = 0; t < 8; ++t) ac[t] = (f32x4){0.f, 0.f, 0.f, 0.f};
  asm volatile("s_nop 1" : "+v"(ac[0]), "+v"(ac[1]), "+v"(ac[2]), "+v"(ac[3]), "+v"(ac[4]), "+v"(ac[5]), "+v"(ac[6]), "+v"(ac[7]));
  bf16x8 uf[2][8], kf[3];
  auto rdu = [&](int r, int dl) LAM { uf[r & 1][dl] = lds_b128(smem, uaddr + 64 * (7 - r) + 512 * dl); };
  auto rdk = [&](int tau) LAM {
    const int r = tau >> 3, i = (tau & 1) ? 7 - ((tau & 7) >> 1) : ((tau & 7) >> 1);
    const unsigned o = kaddr + 64 * (7 - r) + 512 * (7 - i);
    const u32x2 k0 = *(const volatile LAS u32x2*)((LAS char*)smem + o), k1 = *(const volatile LAS u32x2*)((LAS char*)smem + o + 8);
    kf[tau % 3] = mk8(k0[0], k0[1], k1[0], k1[1]);
  };
#pragma unroll
  for (int dl = 0; dl < 8; ++dl) rdu(0, dl);
  rdk(0); rdk(1);
  sfor<0, 64>([&](auto S) LAM {
    constexpr int tau = S.v, r = tau >> 3, i = (tau & 1) ? 7 - ((tau & 7) >> 1) : ((tau & 7) >> 1);
    if constexpr (tau + 2 < 64) rdk(tau + 2);
    if constexpr (r < 7) rdu(r + 1, tau & 7);
#pragma unroll
    for (int t = i; t < 8; ++t) mfma_ip(ac[t], kf[tau % 3], uf[r & 1][t - i]);
    SCHED_BAR();
  });
  asm volatile("s_nop 7" : "+v"(ac[0]), "+v"(ac[1]), "+v"(ac[2]), "+v"(ac[3]), "+v"(ac[4]), "+v"(ac[5]), "+v"(ac[6]), "+v"(ac[7]));
  const rsrc_t ra = mk_rsrc(ws + WS_AT, SL * DM * 2);
#pragma unroll
  for (int t = 0; t < 8; ++t) {
    const int l0 = 256 * t + 16 * p + 4 * g4;
    const u32x2 uu = lds_b64(smem, base + CV_U + 512 + 2 * l0);
    u32x2 o;
    o[0] = pk2(__builtin_fmaf(bflo(uu[0]), dsk, ac[t][0]), __builtin_fmaf(bfhi(uu[0]), dsk, ac[t][1]));
    o[1] = pk2(__builtin_fmaf(bflo(uu[1]), dsk, ac[t][2]), __builtin_fmaf(bfhi(uu[1]), dsk, ac[t][3]));
    st64(ra, (d * SL + l0) * 2, o);
  }
  drain();
}

DEV void s4_role(int j, const u16* __restrict__ x, const u16* __restrict__ wq, const u16* __restrict__ wo, const u16* Are, const u16* Aim, const u16* Bre, const u16* Bim,
                 const u16* Cre, const u16* Cim, const u16* logdt, const u16* Dskip, char* __restrict__ ws, char* smem) {
  const int tid = tidx(), lane = tid & 63;
  const int wv = __builtin_amdgcn_readfirstlane(tid >> 6);
  const int cg = j & 7, idx = j >> 3;
  const unsigned lds0 = (unsigned)(size_t)(LAS char*)smem;
  if (wv == 3) {
    constexpr unsigned XT = 3 * CV_WAVE;
    const u16* xb = x + (size_t)64 * idx * DM + 80 * cg;
#pragma unroll
    for (int q = 0; q < 10; ++q) {
      const int ci = 64 * q + lane, rho = ci / 10, pos = ci - rho * 10;
      const int row = (rho & ~12) | ((rho & 4) << 1) | ((rho & 8) >> 1);
      glds_s(xb, (row * DM + pos * 8) * 2, lds0 + XT + q * 1024);
    }
    const u64 tag = read_tag(ws);
    unsigned pf[2];
    l2_prefetch(x, wq, cg, idx * 64, 3072, pf);
    asm volatile("s_waitcnt vmcnt(2) lgkmcnt(0)" ::: "memory");
    const int g4 = lane >> 4, c16 = lane & 15;
    const unsigned bo = XT + (16 * (g4 >> 1) + 4 * (g4 & 1) + (c16 >> 2)) * 160 + (lane & 3) * 8;
    const rsrc_t ru = mk_rsrc(ws + WS_UT, SL * DM * 2);
#pragma unroll
    for (int hf = 0; hf < 2; ++hf)
#pragma unroll
      for (int n = 0; n < 5; ++n) {
        const bf16x8 v = lds_tr2(smem, bo + hf * 5120 + n * 32, bo + hf * 5120 + 1280 + n * 32);
        st128(ru, ((80 * cg + 16 * n + c16) * SL + 64 * idx + 32 * hf + 8 * g4) * 2, __builtin_bit_cast(u32x4, v));
      }
    drain();
    if (lane == 0) flag_set(ws, WS_FU + (cg * 32 + idx) * 64, tag);
    asm volatile("" ::"v"(pf[0]), "v"(pf[1]));
    bar();
    return;
  }
  const u64 tag = read_tag(ws);
  const int nch = idx < 16 ? 3 : 2;
  const int d0 = 80 * cg + (idx < 16 ? 3 * idx : 48 + 2 * (idx - 16));
  if (wv < nch) s4_wave(d0 + wv, cg, x, wq, Are, Aim, Bre, Bim, Cre, Cim, logdt, Dskip, ws, tag, smem, wv * CV_WAVE);
  if (wv >= nch) {
    unsigned pf[2];
    l2_prefetch(x, wq, cg, (16 + idx) * 64, 3072, pf);
    asm volatile("" ::"v"(pf[0]), "v"(pf[1]));
  }
  bar();
  if (tid == 0) flag_set(ws, WS_FA + (cg * 32 + idx) * 64, tag);
}

extern "C" NOPK __global__ __launch_bounds__(256, 2) void fused_kernel(
    const u16* __restrict__ x, const u16* __restrict__ wqkv, const u16* __restrict__ wo, const u16* __restrict__ gw, const u16* Are,
    const u16* Aim, const u16* Bre, const u16* Bim, const u16* Cre, const u16* Cim, const u16* logdt, const u16* Dskip,
    char* __restrict__ ws, u16* __restrict__ out) {
  extern __shared__ __attribute__((aligned(16))) char smem[];
  const int b = bidx();
  if (b < 256) attn_role(b, x, wqkv, wo, gw, ws, out, smem);
  else s4_role(b - 256, x, wqkv, wo, Are, Aim, Bre, Bim, Cre, Cim, logdt, Dskip, ws, smem);
}

extern "C" void kernel_launch(void* const* d_in, const int* in_sizes, int n_in, void* d_out, int out_size, void* d_ws,
                              size_t ws_size, hipStream_t stream) {
  (void)hipFuncSetAttribute((const void*)fused_kernel, hipFuncAttributeMaxDynamicSharedMemorySize, LDS_BYTES);
  fused_kernel<<<512, 256, LDS_BYTES, stream>>>((const u16*)d_in[0], (const u16*)d_in[10], (const u16*)d_in[11],
                                                (const u16*)d_in[12], (const u16*)d_in[1], (const u16*)d_in[2],
                                                (const u16*)d_in[3], (const u16*)d_in[4], (const u16*)d_in[5],
                                                (const u16*)d_in[6], (const u16*)d_in[7], (const u16*)d_in[8],
                                                (char*)d_ws, (u16*)d_out);
}
```
